```python
import torch

def _softmax(x, axis=-1):
    return torch.softmax(x, dim=axis)
def _gelu(x):
    return torch.nn.functional.gelu(x, approximate="tanh")
def _silu(x):
    return torch.nn.functional.silu(x)
def _sigmoid(x):
    return torch.sigmoid(x)
def _layernorm(x, axis=-1, eps=1e-5):
    m = x.mean(dim=axis, keepdim=True)
    v = x.var(dim=axis, keepdim=True, unbiased=False)
    return (x - m) / torch.sqrt(v + eps)
def _scatter_add(tgt, idx, src, axis=0):
    return tgt.clone().index_add_(axis, idx, src)
def _segment_sum(src, seg, n):
    out = torch.zeros((n,) + src.shape[1:], dtype=src.dtype)
    return out.index_add_(0, seg, src)
def _pad(x, pw):
    flat = tuple(v for pair in reversed(pw) for v in pair)
    return torch.nn.functional.pad(x, flat)

kernel_name = 'interleave_layers__sliding_local_dilated__swiglu_ffn__prompt'

def setup_inputs():

    _w = (lambda a: (lambda t: t.to(torch.bfloat16) if t.is_floating_point() else t)(torch.as_tensor(a)))
    def _A_setup():
        B, S, D = 1, 2048, 576
        return {
            "x": _w((lambda *s: torch.randn(*s, dtype=torch.bfloat16) * 0.05)(B, S, D)),
            "wqkv": _w((lambda *s: torch.randn(*s, dtype=torch.bfloat16) * 0.05)(D, 3 * D)),
            "wo": _w((lambda *s: torch.randn(*s, dtype=torch.bfloat16) * 0.05)(D, D)),
        }
    _A_in = _A_setup()

    def _B_setup():
        B, S, D, F = 1, 2048, 576, 4608
        return {
            "x": _w((lambda *s: torch.randn(*s, dtype=torch.bfloat16) * 0.05)(B, S, D)),
            "w_gate": _w((lambda *s: torch.randn(*s, dtype=torch.bfloat16) * 0.05)(D, F)),
            "w_up": _w((lambda *s: torch.randn(*s, dtype=torch.bfloat16) * 0.05)(D, F)),
            "w_down": _w((lambda *s: torch.randn(*s, dtype=torch.bfloat16) * 0.05)(F, D)),
        }
    _B_in = _B_setup()

    return {
        "A_x": _A_in["x"],
        "A_wqkv": _A_in["wqkv"],
        "A_wo": _A_in["wo"],
        "B_x": _B_in["x"],
        "B_w_gate": _B_in["w_gate"],
        "B_w_up": _B_in["w_up"],
        "B_w_down": _B_in["w_down"],
    }

def reference(A_x, A_wqkv, A_wo, B_x, B_w_gate, B_w_up, B_w_down):
    def _A_ref(x, wqkv, wo):
        B, S, D = x.shape
        H = 32; Dh = D // H
        W, dil = 8, 2
        qkv = (lambda x, *d: x.permute(*d))((x @ wqkv).reshape(B, S, 3, H, Dh), 2, 0, 3, 1, 4)
        q, k, v = qkv[0], qkv[1], qkv[2]
        scale = Dh ** -0.5
        pos = torch.arange(S)
        off = torch.arange(W) * dil
        idx = pos[:, None] - off[None, :]
        mask = idx >= 0
        idx_c = torch.where(mask, idx, 0).reshape(-1)
        kg = k[:, :, idx_c, :].reshape(B, H, S, W, Dh)
        vg = v[:, :, idx_c, :].reshape(B, H, S, W, Dh)
        logits = torch.einsum("bhsd,bhswd->bhsw", q, kg) * scale
        logits = torch.where(mask[None, None, :, :], logits, -1e9)
        attn = _softmax(logits, axis=-1)
        o = torch.einsum("bhsw,bhswd->bhsd", attn, vg)
        return (lambda x, *d: x.permute(*d))(o, 0, 2, 1, 3).reshape(B, S, D) @ wo

    def _B_ref(x, w_gate, w_up, w_down):
        g = _silu(x @ w_gate)
        u = x @ w_up
        return (g * u) @ w_down

    x = A_x
    x = x + _A_ref(_layernorm(x, axis=-1), A_wqkv, A_wo)
    x = x + _B_ref(_layernorm(x, axis=-1), B_w_gate, B_w_up, B_w_down)
    x = x + _A_ref(_layernorm(x, axis=-1), A_wqkv, A_wo)
    x = x + _B_ref(_layernorm(x, axis=-1), B_w_gate, B_w_up, B_w_down)
    return x
```

```cpp
#include <hip/hip_runtime.h>
#include <hip/hip_bf16.h>
typedef __hip_bfloat16 bf16;
#define ROUND_MODE 1
__device__ __forceinline__ float rb(float x){
  return __bfloat162float(__float2bfloat16(x));
}
constexpr int S=2048,D=576,F=4608,H=32,DH=18;
__global__ void ln_k(const bf16* x, bf16* y){
  int row=blockIdx.x; int t=threadIdx.x;
  float v[9]; float s=0;
  for(int i=0;i<9;i++){ v[i]=__bfloat162float(x[row*D+t+64*i]); s+=v[i]; }
  for(int o=32;o>0;o>>=1) s+=__shfl_xor(s,o);
  float mean=s/D; float q=0;
  for(int i=0;i<9;i++){ float d=v[i]-mean; q+=d*d; }
  for(int o=32;o>0;o>>=1) q+=__shfl_xor(q,o);
  float var=q/D;
  float mb=rb(mean), vb=rb(var);
  float den=rb(sqrtf(rb(vb+1e-5f)));
  for(int i=0;i<9;i++){ y[row*D+t+64*i]=__float2bfloat16(rb(v[i]-mb)/den); }
}
template<int MODE>
__global__ void gemm_k(const bf16* A,const bf16* B,bf16* C,const bf16* res,int M,int N,int K){
  __shared__ float sa[16][17], sb[16][17];
  int tx=threadIdx.x, ty=threadIdx.y; int row=blockIdx.y*16+ty, col=blockIdx.x*16+tx;
  float acc=0;
  for(int k0=0;k0<K;k0+=16){
    sa[ty][tx]=__bfloat162float(A[(size_t)row*K+k0+tx]);
    sb[ty][tx]=__bfloat162float(B[(size_t)(k0+ty)*N+col]);
    __syncthreads();
    for(int k=0;k<16;k++) acc+=sa[ty][k]*sb[k][tx];
    __syncthreads();
  }
  if(MODE==0) C[(size_t)row*N+col]=__float2bfloat16(acc);
  else C[(size_t)row*N+col]=__float2bfloat16(__bfloat162float(res[(size_t)row*N+col])+rb(acc));
}
__global__ void attn_k(const bf16* qkv, bf16* o){
  int idx=blockIdx.x*blockDim.x+threadIdx.x; int s=idx/H, h=idx%H;
  float q[DH]; for(int d=0;d<DH;d++) q[d]=__bfloat162float(qkv[(size_t)s*3*D+h*DH+d]);
  float lg[8]; float mx=-1e30f;
  const float scale=rb(0.23570226039551584f);
  for(int w=0;w<8;w++){ int j=s-2*w; float a;
    if(j>=0){ a=0; for(int d=0;d<DH;d++) a+=q[d]*__bfloat162float(qkv[(size_t)j*3*D+D+h*DH+d]); a=rb(rb(a)*0.23570226039551584f);} else a=rb(-1e9f);
    lg[w]=a; mx=fmaxf(mx,a);}
  (void)scale;
  float sum=0; for(int w=0;w<8;w++){ lg[w]=__expf(lg[w]-mx); sum+=lg[w]; }
  float out[DH]; for(int d=0;d<DH;d++) out[d]=0;
  for(int w=0;w<8;w++){ int j=s-2*w; if(j<0) j=0; float p=rb(lg[w]/sum); for(int d=0;d<DH;d++) out[d]+=p*__bfloat162float(qkv[(size_t)j*3*D+2*D+h*DH+d]); }
  for(int d=0;d<DH;d++) o[(size_t)s*D+h*DH+d]=__float2bfloat16(out[d]);
}
__global__ void glu_k(const bf16* g,const bf16* u,bf16* h,int n){
  int i=blockIdx.x*blockDim.x+threadIdx.x; if(i>=n) return;
  float gv=__bfloat162float(g[i]); float s=rb(gv/(1.f+__expf(-gv)));
  h[i]=__float2bfloat16(s*__bfloat162float(u[i]));
}
extern "C" void kernel_launch(void* const* d_in, const int* in_sizes, int n_in, void* d_out, int out_size, void* d_ws, size_t ws_size, hipStream_t stream){
  const bf16* x=(const bf16*)d_in[0]; const bf16* wqkv=(const bf16*)d_in[1]; const bf16* wo=(const bf16*)d_in[2];
  const bf16* wg=(const bf16*)d_in[4]; const bf16* wu=(const bf16*)d_in[5]; const bf16* wd=(const bf16*)d_in[6];
  char* ws=(char*)d_ws;
  bf16* ln=(bf16*)ws; ws+=S*D*2;
  bf16* qkv=(bf16*)ws; ws+=S*3*D*2;
  bf16* o=(bf16*)ws; ws+=S*D*2;
  bf16* x1=(bf16*)ws; ws+=S*D*2;
  bf16* x2=(bf16*)ws; ws+=S*D*2;
  bf16* g=(bf16*)ws; ws+=(size_t)S*F*2;
  bf16* u=(bf16*)ws; ws+=(size_t)S*F*2;
  bf16* hh=(bf16*)ws; ws+=(size_t)S*F*2;
  const bf16* cur=x;
  for(int l=0;l<2;l++){
    ln_k<<<S,64,0,stream>>>(cur,ln);
    gemm_k<0><<<dim3(3*D/16,S/16),dim3(16,16),0,stream>>>(ln,wqkv,qkv,nullptr,S,3*D,D);
    attn_k<<<S*H/256,256,0,stream>>>(qkv,o);
    gemm_k<1><<<dim3(D/16,S/16),dim3(16,16),0,stream>>>(o,wo,x1,cur,S,D,D);
    ln_k<<<S,64,0,stream>>>(x1,ln);
    gemm_k<0><<<dim3(F/16,S/16),dim3(16,16),0,stream>>>(ln,wg,g,nullptr,S,F,D);
    gemm_k<0><<<dim3(F/16,S/16),dim3(16,16),0,stream>>>(ln,wu,u,nullptr,S,F,D);
    glu_k<<<S*F/256,256,0,stream>>>(g,u,hh,S*F);
    bf16* dst = l==0? x2 : (bf16*)d_out;
    gemm_k<1><<<dim3(D/16,S/16),dim3(16,16),0,stream>>>(hh,wd,dst,x1,S,D,F);
    cur=x2;
  }
}
```

```cpp
#define NLAYERS 2
#include <hip/hip_runtime.h>
#include <hip/hip_bf16.h>
#include <type_traits>

typedef unsigned short u16;
typedef short bf16x8 __attribute__((ext_vector_type(8)));
typedef short bf16x4 __attribute__((ext_vector_type(4)));
typedef float f32x4 __attribute__((ext_vector_type(4)));
typedef float f32x2 __attribute__((ext_vector_type(2)));
typedef unsigned u32x4 __attribute__((ext_vector_type(4)));
typedef unsigned u32x2 __attribute__((ext_vector_type(2)));
typedef __bf16 bf2_t __attribute__((ext_vector_type(2)));
#define LAS __attribute__((address_space(3)))
typedef LAS unsigned char* ldsp;

constexpr int S_ = 2048, D_ = 576, F_ = 4608;

#define WAIT_V(n) asm volatile("s_waitcnt vmcnt(" #n ")" ::: "memory")
#define WAIT_L0() asm volatile("s_waitcnt lgkmcnt(0)" ::: "memory")
#define BAR() do { asm volatile("" ::: "memory"); __builtin_amdgcn_s_barrier(); asm volatile("" ::: "memory"); } while (0)
#define SCHED() __builtin_amdgcn_sched_barrier(0)
#define MFMA(a, b, c) __builtin_amdgcn_mfma_f32_16x16x32_bf16(a, b, c, 0, 0, 0)

__device__ __forceinline__ void glds16(const void* sbase, unsigned voff, unsigned ldst) {
  asm volatile("s_mov_b32 m0, %2\n\ts_nop 0\n\tglobal_load_lds_dwordx4 %0, %1" :: "v"(voff), "s"(sbase), "s"(ldst) : "memory");
}
__device__ __forceinline__ bf16x8 lds_b128(ldsp p) { return *(const LAS bf16x8*)p; }
__device__ __forceinline__ bf16x4 lds_tr(ldsp p) { return __builtin_amdgcn_ds_read_tr16_b64_v4i16((LAS bf16x4*)p); }
__device__ __forceinline__ bf16x8 cat8(bf16x4 a, bf16x4 b) { return __builtin_shufflevector(a, b, 0, 1, 2, 3, 4, 5, 6, 7); }
__device__ __forceinline__ unsigned pk2(float lo, float hi) {
  bf2_t v; v[0] = (__bf16)lo; v[1] = (__bf16)hi; return __builtin_bit_cast(unsigned, v);
}
__device__ __forceinline__ float dot2(unsigned a, unsigned b, float c) {
  return __builtin_amdgcn_fdot2_f32_bf16(__builtin_bit_cast(bf2_t, a), __builtin_bit_cast(bf2_t, b), c, false);
}
__device__ __forceinline__ float rb(float x) { return (float)(__bf16)x; }
__device__ __forceinline__ float lo16(unsigned u) { return __uint_as_float(u << 16); }
__device__ __forceinline__ float hi16(unsigned u) { return __uint_as_float(u & 0xffff0000u); }
__device__ __forceinline__ f32x2 ln_scalars(float mean, float var) {
  float mb = rb(mean), vb = rb(var);
  float den = rb(sqrtf(rb(vb + 1e-5f)));
  return (f32x2){mb, 1.0f / den};
}

constexpr int K1_AS = 10240, K1_WS = 27648, K1_ST = K1_AS + K1_WS;
constexpr int K1_STAT = 4 * K1_ST;
constexpr int K1_LDS = K1_STAT + 1024;
constexpr int K1_QROW = 592;
constexpr int K1_OOFF = 80 * K1_QROW;

__global__ __launch_bounds__(512, 2) void k1_qkv_attn(const u16* __restrict__ x, const u16* __restrict__ wqkv, u16* __restrict__ o) {
  extern __shared__ __attribute__((aligned(16))) unsigned char smem_[];
  ldsp lds = (ldsp)smem_;
  const unsigned lbase = (unsigned)(size_t)lds;
  const int tid = threadIdx.x, lane = tid & 63, wave = __builtin_amdgcn_readfirstlane(tid >> 6);
  const int bid = blockIdx.x, xcd = bid & 7, idx = bid >> 3;
  const int mt = (xcd >> 1) * 8 + (idx & 7), hg = (xcd & 1) * 4 + (idx >> 3);
  const int m0 = mt * 64;
  unsigned soff[5];
#pragma unroll
  for (int i = 0; i < 5; ++i) {
    const int pc = wave + 8 * i;
    if (pc < 10) {
      int row = 8 * pc + (lane >> 3);
      int ch = (lane & 7) ^ ((row >> 1) & 7);
      int gr = m0 - 16 + row; gr = gr < 0 ? 0 : gr;
      soff[i] = (unsigned)(gr * 1152 + ch * 16);
    } else {
      int b = (pc - 10) * 1024 + lane * 16;
      int pr = b / 864, rem = b % 864, t = rem / 432, c = (rem % 432) >> 4;
      int k = (pr >> 4) * 32 + ((pr >> 2) & 3) * 8 + t * 4 + (pr & 3);
      int seg = c / 9, cc = c % 9;
      soff[i] = (unsigned)(k * 3456 + (seg * 576 + hg * 72 + cc * 8) * 2);
    }
  }
  auto stage = [&](int kt) {
    const int slot = (kt & 3) * K1_ST;
    const char* ab = (const char*)x + kt * 128;
    const char* wb = (const char*)wqkv + (size_t)kt * 64 * 3456;
#pragma unroll
    for (int i = 0; i < 5; ++i) {
      const int pc = wave + 8 * i;
      if (pc < 37) {
        glds16(pc < 10 ? ab : wb, soff[i], lbase + slot + pc * 1024);
      }
    }
  };
  stage(0); stage(1); stage(2);
  const int fr = lane & 15, g4 = lane >> 4;
  const int abase = fr * 128 + ((g4 ^ ((fr >> 1) & 7)) << 4);
  const int wbase = K1_AS + (lane >> 2) * 864 + (lane & 3) * 8 + wave * 64;
  f32x4 acc[5][2], cs[2];
#pragma unroll
  for (int i = 0; i < 5; ++i) { acc[i][0] = (f32x4){0, 0, 0, 0}; acc[i][1] = (f32x4){0, 0, 0, 0}; }
  cs[0] = (f32x4){0, 0, 0, 0}; cs[1] = (f32x4){0, 0, 0, 0};
  const bf16x8 ones = {0x3f80, 0x3f80, 0x3f80, 0x3f80, 0x3f80, 0x3f80, 0x3f80, 0x3f80};
#pragma unroll
  for (int kt = 0; kt < 9; ++kt) {
    if (kt < 7) { if (wave < 5) WAIT_V(10); else WAIT_V(8); }
    else if (kt == 7) { if (wave < 5) WAIT_V(5); else WAIT_V(4); }
    else WAIT_V(0);
    BAR();
    if (kt + 3 < 9) stage(kt + 3);
    const int slot = (kt & 3) * K1_ST;
#pragma unroll
    for (int kk = 0; kk < 2; ++kk) {
      bf16x8 xf[5];
#pragma unroll
      for (int i = 0; i < 5; ++i) xf[i] = lds_b128(lds + slot + ((abase ^ (kk * 64)) + i * 2048));
      if (wave < 7) {
        bf16x8 wf[2];
#pragma unroll
        for (int T = 0; T < 2; ++T)
          wf[T] = cat8(lds_tr(lds + slot + wbase + kk * 16 * 864 + T * 32), lds_tr(lds + slot + wbase + kk * 16 * 864 + 432 + T * 32));
#pragma unroll
        for (int T = 0; T < 2; ++T) {
#pragma unroll
          for (int i = 0; i < 5; ++i) acc[i][T] = MFMA(wf[T], xf[i], acc[i][T]);
          cs[T] = MFMA(wf[T], ones, cs[T]);
        }
      } else {
#pragma unroll
        for (int i = 0; i < 5; ++i) { acc[i][0] = MFMA(ones, xf[i], acc[i][0]); acc[i][1] = MFMA(xf[i], xf[i], acc[i][1]); }
      }
    }
    WAIT_L0();
  }
  if (wave == 7) {
#pragma unroll
    for (int i = 0; i < 5; ++i) {
      const int r = fr & 3;
      float s2 = r == 0 ? acc[i][1][0] : r == 1 ? acc[i][1][1] : r == 2 ? acc[i][1][2] : acc[i][1][3];
      float mean = acc[i][0][0] * (1.0f / 576.0f);
      float var = fmaxf(s2 * (1.0f / 576.0f) - mean * mean, 0.0f);
      f32x2 sc = ln_scalars(mean, var);
      if (g4 == (fr >> 2)) *(LAS f32x2*)(lds + K1_STAT + (i * 16 + fr) * 8) = sc;
    }
  }
  WAIT_L0();
  BAR();
  if (wave < 7) {
#pragma unroll
    for (int i = 0; i < 5; ++i) {
      const f32x2 sc = *(LAS f32x2*)(lds + K1_STAT + (i * 16 + fr) * 8);
#pragma unroll
      for (int T = 0; T < 2; ++T) {
        const int vc = (wave * 2 + T) * 16 + g4 * 4;
        f32x4 v = (acc[i][T] - cs[T] * sc[0]) * sc[1];
#pragma unroll
        for (int hh = 0; hh < 2; ++hh) {
          const int c = vc + hh * 2;
          if (c < 216) {
            const int hd = c / 18, e = c % 18;
            *(LAS unsigned*)(lds + (i * 16 + fr) * K1_QROW + hd * 48 + e * 2) = pk2(v[hh * 2], v[hh * 2 + 1]);
          }
        }
      }
    }
  }
  WAIT_L0();
  BAR();
  if (tid < 256) {
    const int r = tid >> 2, h = tid & 3;
    unsigned q[9];
    {
      ldsp p = lds + (16 + r) * K1_QROW + h * 48;
      u32x4 a = *(LAS u32x4*)p, b = *(LAS u32x4*)(p + 16);
      q[0] = a[0]; q[1] = a[1]; q[2] = a[2]; q[3] = a[3]; q[4] = b[0]; q[5] = b[1]; q[6] = b[2]; q[7] = b[3];
      q[8] = *(LAS unsigned*)(p + 32);
    }
    float lg[8];
#pragma unroll
    for (int w = 0; w < 8; ++w) {
      ldsp p = lds + (16 + r - 2 * w) * K1_QROW + 192 + h * 48;
      u32x4 a = *(LAS u32x4*)p, b = *(LAS u32x4*)(p + 16);
      unsigned c = *(LAS unsigned*)(p + 32);
      float d = 0.f;
#pragma unroll
      for (int j = 0; j < 4; ++j) d = dot2(q[j], a[j], d);
#pragma unroll
      for (int j = 0; j < 4; ++j) d = dot2(q[4 + j], b[j], d);
      d = dot2(q[8], c, d);
      d = rb(rb(d) * 0.23570226039551584f);
      lg[w] = (m0 + r - 2 * w >= 0) ? d : -1e9f;
    }
    float mx = lg[0];
#pragma unroll
    for (int w = 1; w < 8; ++w) mx = fmaxf(mx, lg[w]);
    float sum = 0.f;
#pragma unroll
    for (int w = 0; w < 8; ++w) { lg[w] = __expf(lg[w] - mx); sum += lg[w]; }
    const float rs = 1.0f / sum;
    float ov[18];
#pragma unroll
    for (int d = 0; d < 18; ++d) ov[d] = 0.f;
#pragma unroll
    for (int w = 0; w < 8; ++w) {
      const float p = rb(lg[w] * rs);
      ldsp pp = lds + (16 + r - 2 * w) * K1_QROW + 384 + h * 48;
      u32x4 a = *(LAS u32x4*)pp, b = *(LAS u32x4*)(pp + 16);
      unsigned c = *(LAS unsigned*)(pp + 32);
#pragma unroll
      for (int j = 0; j < 4; ++j) { ov[2 * j] += p * lo16(a[j]); ov[2 * j + 1] += p * hi16(a[j]); }
#pragma unroll
      for (int j = 0; j < 4; ++j) { ov[8 + 2 * j] += p * lo16(b[j]); ov[8 + 2 * j + 1] += p * hi16(b[j]); }
      ov[16] += p * lo16(c); ov[17] += p * hi16(c);
    }
#pragma unroll
    for (int j = 0; j < 9; ++j) *(LAS unsigned*)(lds + K1_OOFF + r * 144 + h * 36 + j * 4) = pk2(ov[2 * j], ov[2 * j + 1]);
  }
  WAIT_L0();
  BAR();
  for (int c = tid; c < 576; c += 512) {
    const int r = c / 9, cc = c % 9;
    u32x4 v = *(LAS u32x4*)(lds + K1_OOFF + r * 144 + cc * 16);
    *(u32x4*)((char*)o + (size_t)(m0 + r) * 1152 + hg * 144 + cc * 16) = v;
  }
}

constexpr int K2_WOFF = 73728, K2_LDS = K2_WOFF + 82944 + 1024;

__global__ __launch_bounds__(512, 2) void k2_wo(const u16* __restrict__ o, const u16* __restrict__ wo, const u16* __restrict__ xres,
                                                u16* __restrict__ x1, f32x2* __restrict__ stats,
                                                const u16* __restrict__ wg, const u16* __restrict__ wu, float* __restrict__ csum, int do_cs) {
  extern __shared__ __attribute__((aligned(16))) unsigned char smem_[];
  ldsp lds = (ldsp)smem_;
  const unsigned lbase = (unsigned)(size_t)lds;
  const int tid = threadIdx.x, lane = tid & 63, wave = __builtin_amdgcn_readfirstlane(tid >> 6);
  const int bid = blockIdx.x, xcd = bid & 7, idx = bid >> 3;
  const int mt = xcd * 4 + (idx & 3), nt = idx >> 2;
  const int m0 = mt * 64, n0 = nt * 72;
#pragma unroll
  for (int i = 0; i < 9; ++i) {
    const int row = 8 * wave + (lane >> 3);
    const int ch = (lane & 7) ^ ((row >> 1) & 7);
    glds16((const char*)o + i * 128, (unsigned)((m0 + row) * 1152 + ch * 16), lbase + i * 8192 + wave * 1024);
  }
#pragma unroll
  for (int i = 0; i < 11; ++i) {
    const int pc = wave + 8 * i;
    if (pc < 81) {
      const int b = pc * 1024 + lane * 16;
      const int pr = b / 288, rem = b % 288, t = rem / 144, c = (rem % 144) >> 4;
      const int k = (pr >> 4) * 32 + ((pr >> 2) & 3) * 8 + t * 4 + (pr & 3);
      glds16((const char*)wo, (unsigned)(k * 1152 + n0 * 2 + c * 16), lbase + K2_WOFF + pc * 1024);
    }
  }
  const int fr = lane & 15, g4 = lane >> 4, wm = wave & 3, kh = wave >> 2;
  float csa[8];
#pragma unroll
  for (int j = 0; j < 8; ++j) csa[j] = 0.f;
  const int cs_t = bid >> 7, cs_cg = (bid >> 1) & 63, cs_kh = bid & 1;
  if (do_cs && tid < 432) {
    const u16* w = cs_t ? wu : wg;
    const int cc = tid % 9, rg = tid / 9;
    u32x4 v[6];
#pragma unroll
    for (int r = 0; r < 6; ++r) v[r] = *(const u32x4*)((const char*)w + (size_t)(cs_kh * 288 + rg + 48 * r) * 9216 + cs_cg * 144 + cc * 16);
#pragma unroll
    for (int r = 0; r < 6; ++r)
#pragma unroll
      for (int j = 0; j < 4; ++j) { csa[2 * j] += lo16(v[r][j]); csa[2 * j + 1] += hi16(v[r][j]); }
  }
  u32x2 res[5];
  const int row = m0 + wm * 16 + fr;
  if (kh == 0) {
#pragma unroll
    for (int T = 0; T < 5; ++T) {
      const int c = (T < 4 || g4 < 2) ? (n0 + T * 16 + g4 * 4) : n0;
      res[T] = *(const u32x2*)((const char*)xres + (size_t)row * 1152 + c * 2);
    }
  }
  WAIT_V(0);
  BAR();
  f32x4 acc[5];
#pragma unroll
  for (int T = 0; T < 5; ++T) acc[T] = (f32x4){0, 0, 0, 0};
  const int abase = (wm * 16 + fr) * 128 + ((g4 ^ ((fr >> 1) & 7)) << 4);
  const int wbase = K2_WOFF + (lane >> 2) * 288 + (lane & 3) * 8;
#pragma unroll
  for (int s = 0; s < 9; ++s) {
    const int kb = kh * 9 + s;
    bf16x8 xf = lds_b128(lds + (kb >> 1) * 8192 + (abase ^ ((kb & 1) * 64)));
#pragma unroll
    for (int T = 0; T < 5; ++T) {
      bf16x8 wf = cat8(lds_tr(lds + wbase + kb * 4608 + T * 32), lds_tr(lds + wbase + kb * 4608 + 144 + T * 32));
      acc[T] = MFMA(wf, xf, acc[T]);
    }
  }
  WAIT_L0();
  BAR();
  if (kh == 1) {
#pragma unroll
    for (int T = 0; T < 5; ++T) *(LAS f32x4*)(lds + (wm * 5 + T) * 1024 + lane * 16) = acc[T];
  }
  if (do_cs && tid < 432) {
    const int cc = tid % 9, rg = tid / 9;
    *(LAS f32x4*)(lds + 32768 + (rg * 72 + cc * 8) * 4) = (f32x4){csa[0], csa[1], csa[2], csa[3]};
    *(LAS f32x4*)(lds + 32768 + (rg * 72 + cc * 8 + 4) * 4) = (f32x4){csa[4], csa[5], csa[6], csa[7]};
  }
  WAIT_L0();
  BAR();
  if (kh == 0) {
    float v[5][4];
    float s = 0.f;
#pragma unroll
    for (int T = 0; T < 5; ++T) {
      f32x4 a = acc[T] + *(LAS f32x4*)(lds + (wm * 5 + T) * 1024 + lane * 16);
      const bool valid = (T < 4 || g4 < 2);
      v[T][0] = rb(lo16(res[T][0]) + rb(a[0])); v[T][1] = rb(hi16(res[T][0]) + rb(a[1]));
      v[T][2] = rb(lo16(res[T][1]) + rb(a[2])); v[T][3] = rb(hi16(res[T][1]) + rb(a[3]));
      if (valid) {
        u32x2 w; w[0] = pk2(v[T][0], v[T][1]); w[1] = pk2(v[T][2], v[T][3]);
        *(u32x2*)((char*)x1 + (size_t)row * 1152 + (n0 + T * 16 + g4 * 4) * 2) = w;
        s += (v[T][0] + v[T][1]) + (v[T][2] + v[T][3]);
      }
    }
    s += __shfl_xor(s, 16); s += __shfl_xor(s, 32);
    const float mean = s * (1.0f / 72.0f);
    float q = 0.f;
#pragma unroll
    for (int T = 0; T < 5; ++T) {
      if (T < 4 || g4 < 2) {
#pragma unroll
        for (int j = 0; j < 4; ++j) { const float d = v[T][j] - mean; q += d * d; }
      }
    }
    q += __shfl_xor(q, 16); q += __shfl_xor(q, 32);
    if (g4 == 0) stats[row * 8 + nt] = (f32x2){mean, q};
  } else if (do_cs && tid >= 256 && tid < 256 + 72) {
    const int c = tid - 256;
    float s = 0.f;
#pragma unroll 8
    for (int rg = 0; rg < 48; ++rg) s += *(LAS float*)(lds + 32768 + (rg * 72 + c) * 4);
    csum[cs_kh * 9216 + cs_t * 4608 + cs_cg * 72 + c] = s;
  }
}

constexpr int K3_XS = 32768, K3_WS = 18464, K3_UOFF = 9216 + 16;
constexpr int K3_WOFF = 2 * K3_XS, K3_STAT = K3_WOFF + 4 * K3_WS, K3_CS = K3_STAT + 2048, K3_LDS = K3_CS + 288 * 4;

__global__ __launch_bounds__(512, 2) void k3_gate_up(const u16* __restrict__ x, const u16* __restrict__ wg, const u16* __restrict__ wu,
                                                     const f32x2* __restrict__ stats, const float* __restrict__ csum, u16* __restrict__ h) {
  extern __shared__ __attribute__((aligned(16))) unsigned char smem_[];
  ldsp lds = (ldsp)smem_;
  const unsigned lbase = (unsigned)(size_t)lds;
  const int tid = threadIdx.x, lane = tid & 63, wave = __builtin_amdgcn_readfirstlane(tid >> 6);
  const int bid = blockIdx.x, xcd = bid & 7, idx = bid >> 3;
  const int mp = (xcd & 1) * 4 + (idx & 3), ft = (xcd >> 1) * 8 + (idx >> 2);
  const int m0 = mp * 256, f0 = ft * 144;
  const int grp = wave >> 2, wm = wave & 3;
  const int fr = lane & 15, g4 = lane >> 4;
  unsigned soff[5];
  if (grp == 0) {
#pragma unroll
    for (int i = 0; i < 2; ++i) {
      const int row = wm * 64 + 8 * i + (lane >> 3);
      const int ch = (lane & 7) ^ ((row >> 1) & 7);
      soff[i] = (unsigned)((m0 + row) * 1152 + ch * 16);
    }
    soff[2] = soff[3] = soff[4] = 0;
  } else {
#pragma unroll
    for (int i = 0; i < 5; ++i) {
      const int pc = (wm & 1) * 4 + i;
      const int b = pc * 1024 + lane * 16;
      const int rho = b / 288, c = (b % 288) >> 4;
      const int k = (rho & 0x13) | ((rho & 4) << 1) | ((rho & 8) >> 1);
      soff[i] = (unsigned)(k * 9216 + f0 * 2 + c * 16);
    }
  }
  const char* wsrc = (const char*)((wm & 2) ? wu : wg);
  const int wdst = K3_WOFF + ((wm & 2) ? K3_UOFF : 0) + (wm & 1) * 4096;
  auto stageX = [&](int kt, int sl) {
    const char* xb = (const char*)x + kt * 128;
#pragma unroll
    for (int i = 0; i < 8; ++i)
      glds16(xb + (i >> 1) * (16 * 1152), soff[i & 1], lbase + sl * K3_XS + (wm * 8 + i) * 1024);
  };
  auto stageW = [&](int jj, int j) {
    const char* wb = wsrc + (size_t)jj * (32 * 9216);
    const int dst = wdst + j * K3_WS;
    if ((wm & 1) == 0) {
#pragma unroll
      for (int i = 0; i < 4; ++i) glds16(wb, soff[i], lbase + dst + i * 1024);
      if ((j & 1) == 0) glds16(wb, soff[4], lbase + dst + 4096);
    } else {
      if ((j & 1) == 1) glds16(wb, soff[0], lbase + dst);
#pragma unroll
      for (int i = 1; i < 5; ++i) glds16(wb, soff[i], lbase + dst + i * 1024);
    }
  };
  if (grp == 0) {
    const f32x4* sp = (const f32x4*)(stats + (size_t)(m0 + tid) * 8);
    f32x4 a = sp[0], b = sp[1], c = sp[2], d = sp[3];
    stageX(0, 0);
    const float mean = ((a[0] + a[2]) + (b[0] + b[2]) + (c[0] + c[2]) + (d[0] + d[2])) * 0.125f;
    float m2 = (a[1] + a[3]) + (b[1] + b[3]) + (c[1] + c[3]) + (d[1] + d[3]);
    float e0 = a[0] - mean, e1 = a[2] - mean, e2 = b[0] - mean, e3 = b[2] - mean, e4 = c[0] - mean, e5 = c[2] - mean, e6 = d[0] - mean, e7 = d[2] - mean;
    m2 += 72.0f * (((e0 * e0 + e1 * e1) + (e2 * e2 + e3 * e3)) + ((e4 * e4 + e5 * e5) + (e6 * e6 + e7 * e7)));
    *(LAS f32x2*)(lds + K3_STAT + tid * 8) = ln_scalars(mean, m2 * (1.0f / 576.0f));
    WAIT_V(0);
  } else {
    const int c = tid - 256;
    float a0 = 0.f, a1 = 0.f, b0 = 0.f, b1 = 0.f;
    { const int t = c / 144, cc = c % 144; a0 = csum[t * 4608 + f0 + cc]; a1 = csum[9216 + t * 4608 + f0 + cc]; }
    if (c < 32) { b0 = csum[4608 + f0 + 112 + c]; b1 = csum[9216 + 4608 + f0 + 112 + c]; }
    stageW(0, 0); stageW(1, 1); stageW(2, 2);
    *(LAS float*)(lds + K3_CS + c * 4) = a0 + a1;
    if (c < 32) *(LAS float*)(lds + K3_CS + (256 + c) * 4) = b0 + b1;
    WAIT_V(9);
  }
  WAIT_L0();
  BAR();
  f32x4 acc[4][9];
#pragma unroll
  for (int i = 0; i < 4; ++i)
#pragma unroll
    for (int T = 0; T < 9; ++T) acc[i][T] = (f32x4){0, 0, 0, 0};
  const int abase = (wm * 64 + fr) * 128 + ((g4 ^ ((fr >> 1) & 7)) << 4);
  const int q4 = (lane >> 2) & 3, p4 = lane & 3;
  const int wrow = ((g4 >> 1) * 16 + (g4 & 1) * 4 + q4) * 288;
  const int wbase = K3_WOFF + wrow + p4 * 8 + (grp ? 160 : 0);
  const int mbase = K3_WOFF + wrow + (p4 & 1) * 8 + (p4 >> 1) * K3_UOFF + (grp ? 144 : 128);
  if (grp == 1) BAR();
  auto phase = [&](auto JM, int j) {
    constexpr int jm = decltype(JM)::value;
    bf16x8 xf[4], wf[9];
    constexpr int xs = ((jm >> 1) & 1) * K3_XS, ws = jm * K3_WS;
#pragma unroll
    for (int i = 0; i < 4; ++i) xf[i] = lds_b128(lds + xs + ((abase ^ ((jm & 1) * 64)) + i * 2048));
#pragma unroll
    for (int T = 0; T < 4; ++T) {
      wf[T] = cat8(lds_tr(lds + ws + wbase + T * 32), lds_tr(lds + ws + wbase + 8 * 288 + T * 32));
      wf[4 + T] = cat8(lds_tr(lds + ws + wbase + K3_UOFF + T * 32), lds_tr(lds + ws + wbase + K3_UOFF + 8 * 288 + T * 32));
    }
    wf[8] = cat8(lds_tr(lds + ws + mbase), lds_tr(lds + ws + mbase + 8 * 288));
    if (grp == 0) {
      if ((jm & 1) == 0 && j < 16) stageX((j >> 1) + 1, ((jm >> 1) + 1) & 1);
    } else {
      if (j < 15) { stageW(j + 3, (jm + 3) & 3); WAIT_V(9); } else WAIT_V(0);
    }
    WAIT_L0();
    BAR();
    SCHED();
    __builtin_amdgcn_s_setprio(1);
#pragma unroll
    for (int T = 0; T < 9; ++T)
#pragma unroll
      for (int i = 0; i < 4; ++i) acc[i][T] = MFMA(wf[T], xf[i], acc[i][T]);
    __builtin_amdgcn_s_setprio(0);
    if (grp == 0 && (jm & 1) == 1) WAIT_V(0);
    BAR();
    SCHED();
  };
#pragma unroll 1
  for (int j = 0; j < 16; j += 4) {
    phase(std::integral_constant<int, 0>{}, j);
    phase(std::integral_constant<int, 1>{}, j + 1);
    phase(std::integral_constant<int, 2>{}, j + 2);
    phase(std::integral_constant<int, 3>{}, j + 3);
  }
  phase(std::integral_constant<int, 0>{}, 16);
  phase(std::integral_constant<int, 1>{}, 17);
  if (grp == 0) BAR();
  {
  int tz = threadIdx.x; asm volatile("" : "+v"(tz));
  const int fr = tz & 15, g4 = (tz >> 4) & 3;
  const int cb = grp ? 80 : 0;
  const int cm = grp ? 72 : 64;
  f32x4 csg[4], csu[4], csm;
#pragma unroll
  for (int T = 0; T < 4; ++T) {
    csg[T] = *(LAS f32x4*)(lds + K3_CS + (cb + T * 16 + g4 * 4) * 4);
    csu[T] = *(LAS f32x4*)(lds + K3_CS + (144 + cb + T * 16 + g4 * 4) * 4);
  }
  csm = *(LAS f32x4*)(lds + K3_CS + ((g4 >> 1) * 144 + cm + (g4 & 1) * 4) * 4);
#pragma unroll
  for (int i = 0; i < 4; ++i) {
    const int r = wm * 64 + i * 16 + fr;
    const f32x2 sc = *(LAS f32x2*)(lds + K3_STAT + r * 8);
    char* hrow = (char*)h + (size_t)(m0 + r) * 9216 + f0 * 2;
    unsigned pk[4][2];
#pragma unroll
    for (int T = 0; T < 4; ++T) {
      f32x4 gv = (acc[i][T] - csg[T] * sc[0]) * sc[1];
      f32x4 uv = (acc[i][4 + T] - csu[T] * sc[0]) * sc[1];
      float hv[4];
#pragma unroll
      for (int e = 0; e < 4; ++e) {
        const float gg = rb(gv[e]);
        const float sg = rb(gg * __builtin_amdgcn_rcpf(1.0f + __expf(-gg)));
        hv[e] = sg * rb(uv[e]);
      }
      pk[T][0] = pk2(hv[0], hv[1]); pk[T][1] = pk2(hv[2], hv[3]);
    }
#pragma unroll
    for (int tp = 0; tp < 2; ++tp) {
      auto r0 = __builtin_amdgcn_permlane16_swap(pk[2 * tp][0], pk[2 * tp + 1][0], false, false);
      auto r1 = __builtin_amdgcn_permlane16_swap(pk[2 * tp][1], pk[2 * tp + 1][1], false, false);
      u32x4 w; w[0] = r0[0]; w[1] = r1[0]; w[2] = r0[1]; w[3] = r1[1];
      const int col = cb + tp * 32 + ((g4 & 1) ? 16 + (g4 - 1) * 4 : g4 * 4);
      *(u32x4*)(hrow + col * 2) = w;
    }
    {
      f32x4 mv = (acc[i][8] - csm * sc[0]) * sc[1];
      float hv[4];
#pragma unroll
      for (int e = 0; e < 4; ++e) {
        const float me = rb(mv[e]);
        const float other = __shfl_xor(me, 32);
        const float sg = rb(me * __builtin_amdgcn_rcpf(1.0f + __expf(-me)));
        hv[e] = sg * other;
      }
      if (g4 < 2) { u32x2 w; w[0] = pk2(hv[0], hv[1]); w[1] = pk2(hv[2], hv[3]); *(u32x2*)(hrow + (cm + g4 * 4) * 2) = w; }
    }
  }
  }
}

constexpr int K4_XS = 16384, K4_ST = K4_XS + 18432, K4_LDS = 4 * K4_ST;

__global__ __launch_bounds__(512, 2) void k4_down(const u16* __restrict__ h, const u16* __restrict__ wd, float* __restrict__ part) {
  extern __shared__ __attribute__((aligned(16))) unsigned char smem_[];
  ldsp lds = (ldsp)smem_;
  const unsigned lbase = (unsigned)(size_t)lds;
  const int tid = threadIdx.x, lane = tid & 63, wave = __builtin_amdgcn_readfirstlane(tid >> 6);
  const int bid = blockIdx.x, xcd = bid & 7, idx = bid >> 3;
  const int ks = xcd >> 1, m4 = (xcd & 1) * 8 + (idx & 7), n4 = idx >> 3;
  const int m0 = m4 * 128, n0 = n4 * 144, k0 = ks * 1152;
  const int grp = wave >> 2, wm = wave & 3;
  const int fr = lane & 15, g4 = lane >> 4;
  unsigned soff[9];
  if (wm < 2) {
#pragma unroll
    for (int i = 0; i < 9; ++i) {
      const int b = i * 1024 + lane * 16;
      const int rho = b / 288, c = (b % 288) >> 4;
      const int k = (rho & 0x13) | ((rho & 4) << 1) | ((rho & 8) >> 1);
      soff[i] = (unsigned)((k0 + wm * 32 + k) * 1152 + n0 * 2 + c * 16);
    }
  } else {
#pragma unroll
    for (int i = 0; i < 8; ++i) {
      const int row = (wm - 2) * 64 + 8 * i + (lane >> 3);
      const int ch = (lane & 7) ^ ((row >> 1) & 7);
      soff[i] = (unsigned)((m0 + row) * 9216 + k0 * 2 + ch * 16);
    }
    soff[8] = 0;
  }
  auto stage = [&](int i) {
    const int slot = (i & 3) * K4_ST;
    if (wm < 2) {
      const char* wb = (const char*)wd + (size_t)i * (64 * 1152);
#pragma unroll
      for (int u = 0; u < 9; ++u) glds16(wb, soff[u], lbase + slot + K4_XS + wm * 9216 + u * 1024);
    } else {
      const char* hb = (const char*)h + i * 128;
#pragma unroll
      for (int u = 0; u < 8; ++u) glds16(hb, soff[u], lbase + slot + (wm - 2) * 8192 + u * 1024);
    }
  };
  if (grp == 1) { stage(0); stage(2); if (wm < 2) WAIT_V(9); else WAIT_V(8); }
  else stage(1);
  BAR();
  f32x4 acc[2][9];
#pragma unroll
  for (int i = 0; i < 2; ++i)
#pragma unroll
    for (int T = 0; T < 9; ++T) acc[i][T] = (f32x4){0, 0, 0, 0};
  const int abase = (wm * 32 + fr) * 128 + ((g4 ^ ((fr >> 1) & 7)) << 4);
  const int q4 = (lane >> 2) & 3, p4 = lane & 3;
  const int wbase = K4_XS + ((g4 >> 1) * 16 + (g4 & 1) * 4 + q4) * 288 + p4 * 8;
  if (grp == 1) BAR();
#pragma unroll
  for (int s = 0; s < 9; ++s) {
    const int slot = (((2 * s) & 3) + grp) * K4_ST;
    bf16x8 xf[2][2], wf[2][9];
#pragma unroll
    for (int kk = 0; kk < 2; ++kk) {
#pragma unroll
      for (int i = 0; i < 2; ++i) xf[kk][i] = lds_b128(lds + slot + ((abase ^ (kk * 64)) + i * 2048));
#pragma unroll
      for (int T = 0; T < 9; ++T)
        wf[kk][T] = cat8(lds_tr(lds + slot + wbase + kk * 9216 + T * 32), lds_tr(lds + slot + wbase + kk * 9216 + 8 * 288 + T * 32));
    }
    if (2 * s + 4 < 18) {
      stage(2 * s + grp + 3);
      if (wm < 2) WAIT_V(9); else WAIT_V(8);
    } else if (2 * s + 3 < 18) {
      if (grp == 0) { stage(17); if (wm < 2) WAIT_V(9); else WAIT_V(8); }
      else WAIT_V(0);
    } else WAIT_V(0);
    WAIT_L0();
    BAR();
    SCHED();
    __builtin_amdgcn_s_setprio(1);
#pragma unroll
    for (int kk = 0; kk < 2; ++kk)
#pragma unroll
      for (int T = 0; T < 9; ++T)
#pragma unroll
        for (int i = 0; i < 2; ++i) acc[i][T] = MFMA(wf[kk][T], xf[kk][i], acc[i][T]);
    __builtin_amdgcn_s_setprio(0);
    BAR();
    SCHED();
  }
  if (grp == 0) BAR();
  if (grp == 1) {
#pragma unroll
    for (int i = 0; i < 2; ++i)
#pragma unroll
      for (int T = 0; T < 9; ++T) *(LAS f32x4*)(lds + ((wm * 18 + i * 9 + T) * 1024 + lane * 16)) = acc[i][T];
  }
  WAIT_L0();
  BAR();
  if (grp == 0) {
#pragma unroll
    for (int i = 0; i < 2; ++i) {
      float* prow = part + ((size_t)ks * 2048 + m0 + wm * 32 + i * 16 + fr) * 576 + n0 + g4 * 4;
#pragma unroll
      for (int T = 0; T < 9; ++T) {
        f32x4 a = acc[i][T] + *(LAS f32x4*)(lds + ((wm * 18 + i * 9 + T) * 1024 + lane * 16));
        *(f32x4*)(prow + T * 16) = a;
      }
    }
  }
}

__global__ __launch_bounds__(256) void k5_reduce(const float* __restrict__ part, const u16* __restrict__ x1, u16* __restrict__ out) {
  const int i = blockIdx.x * 256 + threadIdx.x;
  const f32x4* p = (const f32x4*)part + (size_t)i * 2;
  constexpr size_t PS = (size_t)2048 * 576 / 4;
  f32x4 a0 = p[0], a1 = p[1], b0 = p[PS], b1 = p[PS + 1], c0 = p[2 * PS], c1 = p[2 * PS + 1], d0 = p[3 * PS], d1 = p[3 * PS + 1];
  u32x4 r = *((const u32x4*)x1 + i);
  f32x4 s0 = (a0 + b0) + (c0 + d0), s1 = (a1 + b1) + (c1 + d1);
  u32x4 w;
  w[0] = pk2(lo16(r[0]) + rb(s0[0]), hi16(r[0]) + rb(s0[1]));
  w[1] = pk2(lo16(r[1]) + rb(s0[2]), hi16(r[1]) + rb(s0[3]));
  w[2] = pk2(lo16(r[2]) + rb(s1[0]), hi16(r[2]) + rb(s1[1]));
  w[3] = pk2(lo16(r[3]) + rb(s1[2]), hi16(r[3]) + rb(s1[3]));
  *((u32x4*)out + i) = w;
}

extern "C" void kernel_launch(void* const* d_in, const int* in_sizes, int n_in, void* d_out, int out_size, void* d_ws, size_t ws_size,
                              hipStream_t stream) {
  const u16* x = (const u16*)d_in[0];
  const u16* wqkv = (const u16*)d_in[1];
  const u16* wo = (const u16*)d_in[2];
  const u16* wg = (const u16*)d_in[4];
  const u16* wu = (const u16*)d_in[5];
  const u16* wd = (const u16*)d_in[6];
  char* ws = (char*)d_ws;
  u16* o = (u16*)ws;                 ws += (size_t)S_ * D_ * 2;
  u16* x1 = (u16*)ws;                ws += (size_t)S_ * D_ * 2;
  u16* x2 = (u16*)ws;                ws += (size_t)S_ * D_ * 2;
  u16* h = (u16*)ws;                 ws += (size_t)S_ * F_ * 2;
  float* part = (float*)ws;          ws += (size_t)4 * S_ * D_ * 4;
  f32x2* stats = (f32x2*)ws;         ws += (size_t)S_ * 8 * 8;
  float* csum = (float*)ws;          ws += (size_t)2 * 9216 * 4;
  (void)hipFuncSetAttribute((const void*)k1_qkv_attn, hipFuncAttributeMaxDynamicSharedMemorySize, K1_LDS);
  (void)hipFuncSetAttribute((const void*)k2_wo, hipFuncAttributeMaxDynamicSharedMemorySize, K2_LDS);
  (void)hipFuncSetAttribute((const void*)k3_gate_up, hipFuncAttributeMaxDynamicSharedMemorySize, K3_LDS);
  (void)hipFuncSetAttribute((const void*)k4_down, hipFuncAttributeMaxDynamicSharedMemorySize, K4_LDS);
  const u16* cur = x;
  for (int l = 0; l < NLAYERS; ++l) {
    k1_qkv_attn<<<256, 512, K1_LDS, stream>>>(cur, wqkv, o);
    k2_wo<<<256, 512, K2_LDS, stream>>>(o, wo, cur, x1, stats, wg, wu, csum, l == 0 ? 1 : 0);
    k3_gate_up<<<256, 512, K3_LDS, stream>>>(x1, wg, wu, stats, csum, h);
    k4_down<<<256, 512, K4_LDS, stream>>>(h, wd, part);
    u16* dst = l == 0 ? x2 : (u16*)d_out;
    k5_reduce<<<576, 256, 0, stream>>>(part, x1, dst);
    cur = x2;
  }
}
```

```cpp
#define NLAYERS 2
#include <hip/hip_runtime.h>
#include <hip/hip_bf16.h>
#include <type_traits>

typedef unsigned short u16;
typedef short bf16x8 __attribute__((ext_vector_type(8)));
typedef short bf16x4 __attribute__((ext_vector_type(4)));
typedef float f32x4 __attribute__((ext_vector_type(4)));
typedef float f32x2 __attribute__((ext_vector_type(2)));
typedef unsigned u32x4 __attribute__((ext_vector_type(4)));
typedef unsigned u32x2 __attribute__((ext_vector_type(2)));
typedef __bf16 bf2_t __attribute__((ext_vector_type(2)));
#define LAS __attribute__((address_space(3)))
#define GAS __attribute__((address_space(1)))
typedef LAS unsigned char* ldsp;

#define STAMP(kid, i)

#define WAIT_V(n) asm volatile("s_waitcnt vmcnt(" #n ")" ::: "memory")
#define WAIT_L0() asm volatile("s_waitcnt lgkmcnt(0)" ::: "memory")
#define BAR() do { asm volatile("" ::: "memory"); __builtin_amdgcn_s_barrier(); asm volatile("" ::: "memory"); } while (0)
#define SCHED() __builtin_amdgcn_sched_barrier(0)
#define MFMA(a, b, c) __builtin_amdgcn_mfma_f32_16x16x32_bf16(a, b, c, 0, 0, 0)
#define MFMA_ACC(c, a, b) asm volatile("v_mfma_f32_16x16x32_bf16 %0, %1, %2, %0" : "+v"(c) : "v"(a), "v"(b))
#define MFMA_ACCA MFMA_ACC
#define PINA PIN
#define NOPS() asm volatile("s_nop 7\n\ts_nop 7" ::: "memory")
#define P4_MI 4
#define PIN(x) asm volatile("" : "+v"(x))

#define GNOP "3"
#define GSC " sc1"
__device__ __forceinline__ void glds16(const void* sbase, unsigned voff, unsigned ldst) {
  asm volatile("s_mov_b32 m0, %2\n\ts_nop " GNOP "\n\tglobal_load_lds_dwordx4 %0, %1" :: "v"(voff), "s"(sbase), "s"(ldst) : "memory");
}
__device__ __forceinline__ void glds16c(const void* sbase, unsigned voff, unsigned ldst) {
  asm volatile("s_mov_b32 m0, %2\n\ts_nop " GNOP "\n\tglobal_load_lds_dwordx4 %0, %1" GSC :: "v"(voff), "s"(sbase), "s"(ldst) : "memory");
}
__device__ __forceinline__ bf16x8 lds_b128(ldsp p) { return *(const LAS bf16x8*)p; }
__device__ __forceinline__ bf16x4 lds_tr(ldsp p) { return __builtin_amdgcn_ds_read_tr16_b64_v4i16((LAS bf16x4*)p); }
__device__ __forceinline__ bf16x8 cat8(bf16x4 a, bf16x4 b) { return __builtin_shufflevector(a, b, 0, 1, 2, 3, 4, 5, 6, 7); }
__device__ __forceinline__ unsigned pk2(float lo, float hi) {
  bf2_t v; v[0] = (__bf16)lo; v[1] = (__bf16)hi; return __builtin_bit_cast(unsigned, v);
}
__device__ __forceinline__ float dot2(unsigned a, unsigned b, float c) {
  return __builtin_amdgcn_fdot2_f32_bf16(__builtin_bit_cast(bf2_t, a), __builtin_bit_cast(bf2_t, b), c, false);
}
__device__ __forceinline__ float swap1(float x) {
  return __int_as_float(__builtin_amdgcn_mov_dpp(__float_as_int(x), 0xB1, 0xf, 0xf, true));
}
__device__ __forceinline__ float rb(float x) { return (float)(__bf16)x; }
__device__ __forceinline__ float lo16(unsigned u) { return __uint_as_float(u << 16); }
__device__ __forceinline__ float hi16(unsigned u) { return __uint_as_float(u & 0xffff0000u); }
template <int NKB> __device__ __forceinline__ unsigned frag_off(int row, int col) {
  return (unsigned)(((row >> 4) * NKB + (col >> 5)) * 1024 + ((col >> 3) & 3) * 256 + (row & 15) * 16 + ((col >> 2) & 1) * 8);
}

constexpr unsigned XB = 2048u * 576u * 2u;
constexpr unsigned HB = 2048u * 4608u * 2u;
constexpr unsigned WS_CNT = 0;
constexpr unsigned WS_X0 = WS_CNT + 1024u * 256u;
constexpr unsigned WS_X2 = WS_X0 + XB;
constexpr unsigned WS_O = WS_X2 + XB;
constexpr unsigned WS_X1 = WS_O + 2 * XB;
constexpr unsigned WS_H = WS_X1 + 2 * XB;
constexpr unsigned SLABB = 64u * 16u * 18432u;
constexpr unsigned WS_SLAB = WS_H + 2 * HB;
constexpr unsigned WS_STAT = WS_SLAB + 2 * SLABB;
constexpr unsigned WS_WQ = WS_STAT + 2 * 2048u * 64u;
constexpr unsigned WS_WO = WS_WQ + 8u * 18u * 14336u;
constexpr unsigned WS_WG = WS_WO + 8u * 81u * 1024u;
constexpr unsigned WS_WD = WS_WG + 32u * 18u * 18432u;
constexpr unsigned WS_CSQ = WS_WD + 4u * 144u * 9216u;
constexpr unsigned WS_CSG = WS_CSQ + 18u * 8u * 224u * 4u;
constexpr unsigned WS_END = WS_CSG + 18u * 32u * 288u * 4u;
constexpr int C_S1 = 0, C_S2 = 32, C_S3 = 40, C_S5 = 72, C_S4 = 256;

constexpr int PREP_WLDS = 18432 + 64;
__global__ __launch_bounds__(512) void k_prep(const u16* __restrict__ x, const u16* __restrict__ wqkv, const u16* __restrict__ wo,
                                              const u16* __restrict__ wg, const u16* __restrict__ wu, const u16* __restrict__ wd,
                                              unsigned char* __restrict__ ws) {
  extern __shared__ __attribute__((aligned(16))) unsigned char smem_[];
  const int tid = threadIdx.x, lane = tid & 63, wave = __builtin_amdgcn_readfirstlane(tid >> 6);
  ldsp lds = (ldsp)smem_ + wave * PREP_WLDS;
  const unsigned lb = (unsigned)(size_t)lds;
  const int u = blockIdx.x + 256 * wave;
  if (tid < 4) ((unsigned*)(ws + WS_CNT))[(blockIdx.x * 4 + tid) * 64] = 0u;
  const int g4 = lane >> 4, q4 = (lane >> 2) & 3, p4 = lane & 3, fr = lane & 15;
  bf16x8 ones = {0x3f80, 0x3f80, 0x3f80, 0x3f80, 0x3f80, 0x3f80, 0x3f80, 0x3f80};
  const f32x4 z4 = {0.f, 0.f, 0.f, 0.f};
  if (u < 576) {
    const int ft = u & 31, j = u >> 5;
#pragma unroll
    for (int i = 0; i < 9; ++i) {
      const int b = i * 1024 + lane * 16;
      const int rho = b / 288, c = (b % 288) >> 4;
      const int k = (rho & 0x13) | ((rho & 4) << 1) | ((rho & 8) >> 1);
      const unsigned so = (unsigned)((j * 32 + k) * 9216 + ft * 288 + c * 16);
      glds16(wg, so, lb + i * 1024);
      glds16(wu, so, lb + 9232 + i * 1024);
    }
    WAIT_V(0);
    const int wrow = ((g4 >> 1) * 16 + (g4 & 1) * 4 + q4) * 288;
    unsigned char* dst = ws + WS_WG + (size_t)(ft * 18 + j) * 18432 + lane * 16;
    float* cs = (float*)(ws + WS_CSG) + (size_t)(j * 32 + ft) * 288 + g4 * 4;
#pragma unroll
    for (int g = 0; g < 2; ++g)
#pragma unroll
      for (int T = 0; T < 9; ++T) {
        const int o = T < 4 ? wrow + p4 * 8 + (g ? 160 : 0) + T * 32
                    : T < 8 ? wrow + p4 * 8 + (g ? 160 : 0) + 9232 + (T - 4) * 32
                            : wrow + (p4 & 1) * 8 + (p4 >> 1) * 9232 + (g ? 144 : 128);
        const bf16x8 f = cat8(lds_tr(lds + o), lds_tr(lds + o + 8 * 288));
        *(bf16x8*)(dst + (g * 9 + T) * 1024) = f;
        const f32x4 s = MFMA(f, ones, z4);
        if (fr == 0) *(f32x4*)(cs + (g * 9 + T) * 16) = s;
      }
  } else if (u < 864) {
    const int v = u - 576, n4 = v & 3, st = v >> 2;
#pragma unroll
    for (int i = 0; i < 18; ++i) {
      const int b = i * 1024 + lane * 16;
      const int rho = b / 288, c = (b % 288) >> 4;
      const int k = (rho & 0x73) | ((rho & 4) << 1) | ((rho & 8) >> 1);
      glds16(wd, (unsigned)((st * 64 + k) * 1152 + n4 * 288 + c * 16), lb + i * 1024);
    }
    WAIT_V(0);
    const int wrow = ((g4 >> 1) * 16 + (g4 & 1) * 4 + q4) * 288 + p4 * 8;
    unsigned char* dst = ws + WS_WD + (size_t)(n4 * 144 + st * 2) * 9216 + lane * 16;
#pragma unroll
    for (int kk = 0; kk < 2; ++kk)
#pragma unroll
      for (int T = 0; T < 9; ++T) {
        const int o = kk * 9216 + wrow + T * 32;
        *(bf16x8*)(dst + (kk * 9 + T) * 1024) = cat8(lds_tr(lds + o), lds_tr(lds + o + 8 * 288));
      }
  } else if (u < 1008) {
    const int v = u - 864, hg = v & 7, j = v >> 3;
#pragma unroll
    for (int i = 0; i < 14; ++i) {
      int b = i * 1024 + lane * 16; b = b < 13824 ? b : 13824 - 16;
      const int pr = b / 864, rem = b % 864, t = rem / 432, c = (rem % 432) >> 4;
      const int k = ((pr >> 2) & 3) * 8 + t * 4 + (pr & 3);
      const int seg = c / 9, cc = c % 9;
      glds16(wqkv, (unsigned)((j * 32 + k) * 3456 + (seg * 576 + hg * 72 + cc * 8) * 2), lb + i * 1024);
    }
    WAIT_V(0);
    const int wb = (lane >> 2) * 864 + p4 * 8;
    unsigned char* dst = ws + WS_WQ + (size_t)(hg * 18 + j) * 14336 + lane * 16;
    float* cs = (float*)(ws + WS_CSQ) + (size_t)(j * 8 + hg) * 224 + g4 * 4;
#pragma unroll
    for (int T = 0; T < 14; ++T) {
      const bf16x8 f = cat8(lds_tr(lds + wb + T * 32), lds_tr(lds + wb + 432 + T * 32));
      *(bf16x8*)(dst + T * 1024) = f;
      const f32x4 s = MFMA(f, ones, z4);
      if (fr == 0) *(f32x4*)(cs + T * 16) = s;
    }
  } else if (u < 1080) {
    const int v = u - 1008, nt = v & 7, st = v >> 3;
#pragma unroll
    for (int i = 0; i < 9; ++i) {
      const int b = i * 1024 + lane * 16;
      const int pr = b / 288, rem = b % 288, t = rem / 144, c = (rem % 144) >> 4;
      const int k = (pr >> 4) * 32 + ((pr >> 2) & 3) * 8 + t * 4 + (pr & 3);
      glds16(wo, (unsigned)((st * 64 + k) * 1152 + nt * 144 + c * 16), lb + i * 1024);
    }
    WAIT_V(0);
    const int wb = (lane >> 2) * 288 + p4 * 8;
    unsigned char* dst = ws + WS_WO + (size_t)(nt * 9 + st) * 9216 + lane * 16;
#pragma unroll
    for (int kk = 0; kk < 2; ++kk)
#pragma unroll
      for (int T = 0; T < 4; ++T)
        *(bf16x8*)(dst + (kk * 4 + T) * 1024) = cat8(lds_tr(lds + wb + kk * 4608 + T * 32), lds_tr(lds + wb + kk * 4608 + 144 + T * 32));
    {
      const int o = (lane >> 2) * 288 + (p4 & 1) * 8 + (p4 >> 1) * 4608 + 128;
      *(bf16x8*)(dst + 8 * 1024) = cat8(lds_tr(lds + o), lds_tr(lds + o + 144));
    }
  } else if (u < 1208) {
    const int m16 = u - 1080;
    u32x4 v[18];
#pragma unroll
    for (int kb = 0; kb < 18; ++kb) v[kb] = *(const u32x4*)((const char*)x + (size_t)(m16 * 16 + fr) * 1152 + kb * 64 + g4 * 16);
#pragma unroll
    for (int kb = 0; kb < 18; ++kb) *(u32x4*)(ws + WS_X0 + (size_t)(m16 * 18 + kb) * 1024 + lane * 16) = v[kb];
  }
}

constexpr int MK_LDS = 163840;
typedef __amdgpu_buffer_rsrc_t rsrc_t;
__device__ __forceinline__ void st16c(rsrc_t rs, unsigned off, u32x4 v) { __builtin_amdgcn_raw_buffer_store_b128(v, rs, off, 0, 16); }
__device__ __forceinline__ void st8c(rsrc_t rs, unsigned off, u32x2 v) { __builtin_amdgcn_raw_buffer_store_b64(v, rs, off, 0, 16); }
__device__ __forceinline__ u32x4 ld16c(rsrc_t rs, unsigned off) { return __builtin_amdgcn_raw_buffer_load_b128(rs, off, 0, 16); }
__device__ __forceinline__ u32x2 ld8c(rsrc_t rs, unsigned off) { return __builtin_amdgcn_raw_buffer_load_b64(rs, off, 0, 16); }
__device__ __forceinline__ void publish(unsigned char* ws, int line) {
  WAIT_V(0);
  BAR();
  if (threadIdx.x == 0) __hip_atomic_fetch_add((GAS unsigned*)(ws + WS_CNT + line * 256), 1u, __ATOMIC_RELAXED, __HIP_MEMORY_SCOPE_AGENT);
}
__device__ __forceinline__ void poll(unsigned char* ws, int line, unsigned want) {
  unsigned spins = 0;
  while ((unsigned)__builtin_amdgcn_readfirstlane(__hip_atomic_load((GAS unsigned*)(ws + WS_CNT + line * 256), __ATOMIC_RELAXED, __HIP_MEMORY_SCOPE_AGENT)) < want) {
    __builtin_amdgcn_s_sleep(1);
    if (++spins > (1u << 21)) break;
  }
}

constexpr int P1_AS = 10240, P1_ST = P1_AS + 28672, P1_STAT = 4 * P1_ST, P1_CS = 161792;
constexpr int P1_QROW = 592, P1_QOFF = 83968, P1_OOFF = P1_QOFF + 80 * P1_QROW;
constexpr int P2_OOFF = 83968;
constexpr int P3_XS = 16384, P3_WS = 18432, P3_WOFF = 4 * P3_XS, P3_STAT = P3_WOFF + 4 * P3_WS, P3_CS = P1_CS + 896;
constexpr int P4_XS = 16384, P4_ST = P4_XS + 18432, P4_EX = 65536, P4_OWN = 139264;

struct Ctx { unsigned char* ws; ldsp lds; unsigned lbase; int wave; int bid; };

__device__ __forceinline__ void p1_prefetch_w(const Ctx& c, int lane) {
  const int xcd = c.bid & 7, idx = c.bid >> 3, hg = (xcd & 1) * 4 + (idx >> 3);
  const char* wb = (const char*)c.ws + WS_WQ + (size_t)hg * 18 * 14336;
#pragma unroll
  for (int p = 0; p < 3; ++p)
#pragma unroll
    for (int i = 1; i < 5; ++i) {
      const int pc = c.wave + 8 * i;
      if (pc >= 10 && pc < 38) glds16(wb + p * 28672 + (pc - 10) * 1024, lane * 16, c.lbase + p * P1_ST + pc * 1024);
    }
}
template <int N> __device__ __forceinline__ void p2_prefetch_w(const Ctx& c, int lane, int first, int stride) {
  const int xcd = c.bid & 7, idx = c.bid >> 3, nt = (xcd & 1) * 4 + (idx >> 3);
  const char* wb = (const char*)c.ws + WS_WO + (size_t)nt * 82944;
#pragma unroll
  for (int i = 0; i < N; ++i) {
    const int pc = first + stride * i;
    if (pc < 81) glds16(wb + pc * 1024, lane * 16, c.lbase + pc * 1024);
  }
}
template <int N> __device__ __forceinline__ void p3_prefetch_w(const Ctx& c, int lane, int first, int stride) {
  const int xcd = c.bid & 7, idx = c.bid >> 3, ft = (xcd >> 1) * 8 + (idx >> 2);
  const char* wb = (const char*)c.ws + WS_WG + (size_t)ft * 18 * 18432;
#pragma unroll
  for (int i = 0; i < N; ++i) {
    const int pc = first + stride * i;
    if (pc < 54) glds16(wb + pc * 1024, lane * 16, c.lbase + P3_WOFF + pc * 1024);
  }
}
__device__ __forceinline__ void p4_prefetch_w(const Ctx& c, int lane) {
  const int xcd = c.bid & 7, idx = c.bid >> 3, ks = xcd >> 1, n4 = idx >> 3;
  const char* wb = (const char*)c.ws + WS_WD + (size_t)(n4 * 144 + ks * 36) * 9216;
#pragma unroll
  for (int i = 0; i < 7; ++i) {
    const int pc = c.wave + 8 * i;
    if (pc < 54) glds16(wb + pc * 1024, lane * 16, c.lbase + (pc / 18) * P4_ST + P4_XS + (pc % 18) * 1024);
  }
}
__device__ __forceinline__ void signal(unsigned char* ws, int line) {
  BAR();
  if (threadIdx.x == 0) __hip_atomic_fetch_add((GAS unsigned*)(ws + WS_CNT + line * 256), 1u, __ATOMIC_RELAXED, __HIP_MEMORY_SCOPE_AGENT);
}

__device__ __forceinline__ void phase1(const Ctx& c, rsrc_t rs, int l) {
  int tid = threadIdx.x; asm volatile("" : "+v"(tid));
  const int lane = tid & 63, wave = c.wave;
  ldsp lds = c.lds;
  const int xcd = c.bid & 7, idx = c.bid >> 3;
  const int mt = (xcd >> 1) * 8 + (idx & 7), hg = (xcd & 1) * 4 + (idx >> 3);
  const int m0 = mt * 64;
  const char* xb = (const char*)c.ws + (l ? WS_X2 : WS_X0);
  const char* wb = (const char*)c.ws + WS_WQ + (size_t)hg * 18 * 14336;
  STAMP(l * 4 + 0, 0);
  if (l) {
    if (wave == 7) {
      if (mt) poll(c.ws, C_S5 + 2 * mt - 1, 4u);
      poll(c.ws, C_S5 + 2 * mt, 4u);
      poll(c.ws, C_S5 + 2 * mt + 1, 4u);
    }
    BAR();
  }
  STAMP(l * 4 + 0, 1);
  const int am0 = (mt * 4 - 1 + (wave >> 1)) < 0 ? 0 : (mt * 4 - 1 + (wave >> 1));
  const unsigned aoff0 = (unsigned)((am0 * 18 + (wave & 1)) * 1024 + lane * 16);
  const unsigned aoff1 = (unsigned)(((mt * 4 + 3) * 18 + (wave & 1)) * 1024 + lane * 16);
  auto piece = [&](int kt, int i) {
    const int pc = wave + 8 * i;
    const unsigned dst = c.lbase + (kt & 3) * P1_ST + pc * 1024;
    if (i == 0) glds16c(xb + kt * 2048, aoff0, dst);
    else if (pc < 10) glds16c(xb + kt * 2048, aoff1, dst);
    else if (pc < 38) glds16(wb + (size_t)kt * 28672 + (pc - 10) * 1024, lane * 16, dst);
  };
#pragma unroll
  for (int p = 0; p < 3; ++p) { piece(p, 0); if (wave < 2) piece(p, 1); }
  if (l == 0) {
    const int ft = (xcd >> 1) * 8 + (idx >> 2);
    const float* cp = tid < 224 ? (const float*)(c.ws + WS_CSQ) + hg * 224 + tid : (const float*)(c.ws + WS_CSG) + ft * 288 + (tid - 224);
    const int st = tid < 224 ? 8 * 224 : 32 * 288;
    float v[18];
#pragma unroll
    for (int j = 0; j < 18; ++j) v[j] = cp[j * st];
    float s = 0.f;
#pragma unroll
    for (int j = 0; j < 18; ++j) s += v[j];
    *(LAS float*)(lds + P1_CS + tid * 4) = s;
  }
  const int fr = lane & 15, g4 = lane >> 4;
  const int abase = lane * 16;
  const int wbase = P1_AS + (wave < 7 ? wave : 0) * 2048 + lane * 16;
  f32x4 acc[5][2];
#pragma unroll
  for (int i = 0; i < 5; ++i) { acc[i][0] = (f32x4){0, 0, 0, 0}; acc[i][1] = (f32x4){0, 0, 0, 0}; }
  bf16x8 ones = {0x3f80, 0x3f80, 0x3f80, 0x3f80, 0x3f80, 0x3f80, 0x3f80, 0x3f80};
  PIN(ones);
  auto rdX = [&](int j, int i) -> bf16x8 { return lds_b128(lds + ((j >> 1) & 3) * P1_ST + abase + (i * 2 + (j & 1)) * 1024); };
  auto rdW = [&](int j, int T) -> bf16x8 { return lds_b128(lds + ((j >> 1) & 3) * P1_ST + wbase + ((j & 1) * 14 + T) * 1024); };
  WAIT_V(0);
  WAIT_L0();
  BAR();
  STAMP(l * 4 + 0, 2);
  bf16x8 xf[2][5], wf[2][2];
#pragma unroll
  for (int i = 0; i < 5; ++i) xf[0][i] = rdX(0, i);
  wf[0][0] = rdW(0, 0); wf[0][1] = rdW(0, 1);
#pragma unroll
  for (int i = 0; i < 5; ++i) { PIN(acc[i][0]); PIN(acc[i][1]); }
  NOPS();
  SCHED();
  if (wave < 7) {
#pragma unroll
    for (int j = 0; j < 18; ++j) {
      const int cur = j & 1, nx = cur ^ 1;
      if ((j & 1) && j < 17) { if (j < 15) WAIT_V(4); else WAIT_V(0); BAR(); }
      const bool pf = j + 1 < 18, st = (j & 1) && ((j >> 1) + 3 < 9);
      if (pf) { wf[nx][0] = rdW(j + 1, 0); xf[nx][0] = rdX(j + 1, 0); }
      if (st) piece((j >> 1) + 3, 0);
      MFMA_ACC(acc[0][0], wf[cur][0], xf[cur][0]); MFMA_ACC(acc[1][0], wf[cur][0], xf[cur][1]);
      SCHED();
      if (pf) { xf[nx][1] = rdX(j + 1, 1); xf[nx][2] = rdX(j + 1, 2); }
      if (st) piece((j >> 1) + 3, 1);
      MFMA_ACC(acc[2][0], wf[cur][0], xf[cur][2]); MFMA_ACC(acc[3][0], wf[cur][0], xf[cur][3]);
      SCHED();
      if (pf) { xf[nx][3] = rdX(j + 1, 3); xf[nx][4] = rdX(j + 1, 4); }
      if (st) piece((j >> 1) + 3, 2);
      MFMA_ACC(acc[4][0], wf[cur][0], xf[cur][4]); MFMA_ACC(acc[0][1], wf[cur][1], xf[cur][0]);
      SCHED();
      if (pf) { wf[nx][1] = rdW(j + 1, 1); }
      if (st) piece((j >> 1) + 3, 3);
      MFMA_ACC(acc[1][1], wf[cur][1], xf[cur][1]); MFMA_ACC(acc[2][1], wf[cur][1], xf[cur][2]);
      SCHED();
      if (st) piece((j >> 1) + 3, 4);
      MFMA_ACC(acc[3][1], wf[cur][1], xf[cur][3]); MFMA_ACC(acc[4][1], wf[cur][1], xf[cur][4]);
      SCHED();
    }
    NOPS();
    SCHED();
  } else {
    NOPS();
    SCHED();
#pragma unroll
    for (int j = 0; j < 18; ++j) {
      const int cur = j & 1, nx = cur ^ 1;
      if ((j & 1) && j < 17) { if (j < 15) WAIT_V(4); else WAIT_V(0); BAR(); }
      const bool pf = j + 1 < 18, st = (j & 1) && ((j >> 1) + 3 < 9);
      if (pf) {
#pragma unroll
        for (int i = 0; i < 5; ++i) xf[nx][i] = rdX(j + 1, i);
      }
      if (st) {
#pragma unroll
        for (int i = 0; i < 4; ++i) piece((j >> 1) + 3, i);
      }
#pragma unroll
      for (int i = 0; i < 5; ++i) { MFMA_ACC(acc[i][0], ones, xf[cur][i]); MFMA_ACC(acc[i][1], xf[cur][i], xf[cur][i]); }
      SCHED();
    }
    NOPS();
    SCHED();
  }
  STAMP(l * 4 + 0, 3);
  if (wave == 7) {
#pragma unroll
    for (int i = 0; i < 5; ++i) {
      const int r = fr & 3;
      float s2 = r == 0 ? acc[i][1][0] : r == 1 ? acc[i][1][1] : r == 2 ? acc[i][1][2] : acc[i][1][3];
      float mean = acc[i][0][0] * (1.0f / 576.0f);
      float var = fmaxf(s2 * (1.0f / 576.0f) - mean * mean, 0.0f);
      const float den = rb(__builtin_amdgcn_sqrtf(rb(rb(var) + 1e-5f)));
      if (g4 == (fr >> 2)) *(LAS f32x2*)(lds + P1_STAT + (i * 16 + fr) * 8) = (f32x2){rb(mean), __builtin_amdgcn_rcpf(den)};
    }
  }
  WAIT_L0();
  BAR();
  if (wave == 7) p2_prefetch_w<21>(c, lane, 0, 1);
  if (wave < 7) {
    int qo[2][2];
#pragma unroll
    for (int T = 0; T < 2; ++T)
#pragma unroll
      for (int hh = 0; hh < 2; ++hh) {
        const int cc = (wave * 2 + T) * 16 + g4 * 4 + hh * 2;
        qo[T][hh] = P1_QOFF + fr * P1_QROW + (cc < 216 ? (cc / 18) * 48 + (cc % 18) * 2 : 11 * 48 + 40);
      }
    f32x2 scv[5]; f32x4 cs[2];
#pragma unroll
    for (int i = 0; i < 5; ++i) scv[i] = *(LAS f32x2*)(lds + P1_STAT + (i * 16 + fr) * 8);
    cs[0] = *(LAS f32x4*)(lds + P1_CS + (wave * 32 + g4 * 4) * 4); cs[1] = *(LAS f32x4*)(lds + P1_CS + (wave * 32 + 16 + g4 * 4) * 4);
    SCHED();
#pragma unroll
    for (int i = 0; i < 5; ++i) {
      const f32x2 sc = scv[i];
#pragma unroll
      for (int T = 0; T < 2; ++T) {
        const f32x4 v = (acc[i][T] - cs[T] * sc[0]) * sc[1];
        *(LAS unsigned*)(lds + qo[T][0] + i * 16 * P1_QROW) = pk2(v[0], v[1]);
        *(LAS unsigned*)(lds + qo[T][1] + i * 16 * P1_QROW) = pk2(v[2], v[3]);
      }
    }
    p2_prefetch_w<4>(c, lane, 21 + wave, 7);
  }
  WAIT_L0();
  BAR();
  STAMP(l * 4 + 0, 4);
  {
    const int r = (wave >> 2) * 32 + (lane >> 1), h = wave & 3, hf = tid & 1;
    ldsp rb0 = lds + P1_QOFF + (2 + r) * P1_QROW + h * 48;
    ldsp kb = rb0 + 192 + (hf ? 0 : 8 * P1_QROW);
    ldsp vb = rb0 + 384 + hf * 16;
    u32x4 qa = *(LAS u32x4*)(rb0 + 14 * P1_QROW), qb4 = *(LAS u32x4*)(rb0 + 14 * P1_QROW + 16);
    unsigned qc = *(LAS unsigned*)(rb0 + 14 * P1_QROW + 32);
    u32x4 ka[4], kb4[4]; unsigned kc[4];
#pragma unroll
    for (int w = 0; w < 4; ++w) {
      ka[w] = *(LAS u32x4*)(kb + (6 - 2 * w) * P1_QROW); kb4[w] = *(LAS u32x4*)(kb + (6 - 2 * w) * P1_QROW + 16);
      kc[w] = *(LAS unsigned*)(kb + (6 - 2 * w) * P1_QROW + 32);
    }
    SCHED();
    float lg[4] = {0.f, 0.f, 0.f, 0.f};
#pragma unroll
    for (int j = 0; j < 4; ++j)
#pragma unroll
      for (int w = 0; w < 4; ++w) { const unsigned qq = qa[j], kk = ka[w][j]; lg[w] = dot2(qq, kk, lg[w]); }
#pragma unroll
    for (int j = 0; j < 4; ++j)
#pragma unroll
      for (int w = 0; w < 4; ++w) { const unsigned qq = qb4[j], kk = kb4[w][j]; lg[w] = dot2(qq, kk, lg[w]); }
    SCHED();
    u32x4 va[8]; unsigned vc[8];
#pragma unroll
    for (int w = 0; w < 8; ++w) { va[w] = *(LAS u32x4*)(vb + (14 - 2 * w) * P1_QROW); if (w < 6) vc[w] = *(LAS unsigned*)(vb + (14 - 2 * w) * P1_QROW + 16); }
    SCHED();
#pragma unroll
    for (int w = 0; w < 4; ++w) {
      float d = dot2(qc, kc[w], lg[w]);
      d = rb(rb(d) * 0.23570226039551584f);
      lg[w] = (m0 + r - (hf * 8 + 2 * w) >= 0) ? d : -1e9f;
    }
    float mx = fmaxf(fmaxf(lg[0], lg[1]), fmaxf(lg[2], lg[3]));
    mx = fmaxf(mx, swap1(mx));
    float sum = 0.f;
#pragma unroll
    for (int w = 0; w < 4; ++w) { lg[w] = __expf(lg[w] - mx); sum += lg[w]; }
    sum += swap1(sum);
    const float rs_ = __builtin_amdgcn_rcpf(sum);
    float pw[8];
#pragma unroll
    for (int w = 0; w < 4; ++w) {
      const float mine = rb(lg[w] * rs_), other = swap1(mine);
      pw[w] = hf ? other : mine; pw[4 + w] = hf ? mine : other;
    }
    vc[6] = *(LAS unsigned*)(vb + 2 * P1_QROW + 16); vc[7] = *(LAS unsigned*)(vb + 16);
    f32x2 ov[5];
#pragma unroll
    for (int d = 0; d < 5; ++d) ov[d] = (f32x2){0.f, 0.f};
#pragma unroll
    for (int w = 0; w < 8; ++w) {
#pragma unroll
      for (int j = 0; j < 4; ++j) ov[j] += (f32x2){lo16(va[w][j]), hi16(va[w][j])} * pw[w];
      ov[4] += (f32x2){lo16(vc[w]), hi16(vc[w])} * pw[w];
    }
    ldsp ob = lds + P1_OOFF + r * 144 + h * 36 + hf * 16;
#pragma unroll
    for (int j = 0; j < 4; ++j) *(LAS unsigned*)(ob + j * 4) = pk2(ov[j][0], ov[j][1]);
    if (hf) *(LAS unsigned*)(ob + 16) = pk2(ov[4][0], ov[4][1]);
    p2_prefetch_w<4>(c, lane, 49 + wave, 8);
  }
  WAIT_L0();
  BAR();
  STAMP(l * 4 + 0, 5);
  for (int ci = tid; ci < 576; ci += 512) {
    const int cc = ci >> 6, r = ci & 63;
    u32x4 v = *(LAS u32x4*)(lds + P1_OOFF + r * 144 + cc * 16);
    st16c(rs, WS_O + l * XB + frag_off<18>(m0 + r, hg * 72 + cc * 8), v);
  }
  publish(c.ws, l * 512 + C_S1 + mt);
  STAMP(l * 4 + 0, 6);
}

__device__ __forceinline__ void phase2(const Ctx& c, rsrc_t rs, int l) {
  int tid = threadIdx.x; asm volatile("" : "+v"(tid));
  const int lane = tid & 63, wave = c.wave;
  ldsp lds = c.lds;
  const int xcd = c.bid & 7, idx = c.bid >> 3;
  const int mt = (xcd >> 1) * 8 + (idx & 7), nt = (xcd & 1) * 4 + (idx >> 3);
  const int m0 = mt * 64, n0 = nt * 72;
  const int fr = lane & 15, g4 = lane >> 4, wm = wave & 3, kh = wave >> 2;
  STAMP(l * 4 + 1, 0);
  u32x2 res[5];
  const int row = m0 + wm * 16 + fr;
  if (kh == 0) {
#pragma unroll
    for (int T = 0; T < 5; ++T) {
      const int cc = (T < 4 || g4 < 2) ? (n0 + T * 16 + g4 * 4) : n0;
      res[T] = ld8c(rs, (l ? WS_X2 : WS_X0) + frag_off<18>(row, cc));
    }
  }
  if (wave == 7) poll(c.ws, l * 512 + C_S1 + mt, 8u);
  BAR();
  STAMP(l * 4 + 1, 1);
  {
    const char* ob = (const char*)c.ws + WS_O + l * XB + (size_t)mt * 73728;
#pragma unroll
    for (int i = 0; i < 9; ++i) glds16c(ob + (wave + 8 * i) * 1024, lane * 16, c.lbase + P2_OOFF + (wave + 8 * i) * 1024);
  }
  WAIT_V(0);
  BAR();
  STAMP(l * 4 + 1, 2);
  f32x4 acc[5];
#pragma unroll
  for (int T = 0; T < 5; ++T) acc[T] = (f32x4){0, 0, 0, 0};
  {
    const int abase = P2_OOFF + (wm * 18 + kh * 9) * 1024 + lane * 16;
    const int t4 = ((lane & 48) + (lane & 7)) * 16 + 8192;
    bf16x8 xf[9], wf[2][5];
#pragma unroll
    for (int s = 0; s < 9; ++s) xf[s] = lds_b128(lds + abase + s * 1024);
    auto rdW = [&](int s, int T) -> bf16x8 {
      const int kb = kh * 9 + s;
      return T < 4 ? lds_b128(lds + (kb >> 1) * 9216 + ((kb & 1) * 4 + T) * 1024 + lane * 16)
                   : lds_b128(lds + (kb >> 1) * 9216 + t4 + (kb & 1) * 128);
    };
#pragma unroll
    for (int T = 0; T < 5; ++T) wf[0][T] = rdW(0, T);
#pragma unroll
    for (int s = 0; s < 9; ++s) {
      if (s + 1 < 9) {
#pragma unroll
        for (int T = 0; T < 5; ++T) wf[(s + 1) & 1][T] = rdW(s + 1, T);
      }
#pragma unroll
      for (int T = 0; T < 5; ++T) acc[T] = MFMA(wf[s & 1][T], xf[s], acc[T]);
      SCHED();
    }
  }
  WAIT_L0();
  BAR();
  STAMP(l * 4 + 1, 3);
  if (kh == 1) {
#pragma unroll
    for (int T = 0; T < 5; ++T) *(LAS f32x4*)(lds + (wm * 5 + T) * 1024 + lane * 16) = acc[T];
  }
  WAIT_L0();
  BAR();
  if (kh == 1) p3_prefetch_w<14>(c, lane, wm, 4);
  if (kh == 0) {
    float v[5][4];
    float s = 0.f;
    unsigned pk[5][2];
#pragma unroll
    for (int T = 0; T < 5; ++T) {
      f32x4 a = acc[T] + *(LAS f32x4*)(lds + (wm * 5 + T) * 1024 + lane * 16);
      v[T][0] = rb(lo16(res[T][0]) + rb(a[0])); v[T][1] = rb(hi16(res[T][0]) + rb(a[1]));
      v[T][2] = rb(lo16(res[T][1]) + rb(a[2])); v[T][3] = rb(hi16(res[T][1]) + rb(a[3]));
      pk[T][0] = pk2(v[T][0], v[T][1]); pk[T][1] = pk2(v[T][2], v[T][3]);
      if (T < 4 || g4 < 2) s += (v[T][0] + v[T][1]) + (v[T][2] + v[T][3]);
    }
    const unsigned xo = WS_X1 + l * XB;
#pragma unroll
    for (int tp = 0; tp < 2; ++tp) {
      auto r0 = __builtin_amdgcn_permlane16_swap(pk[2 * tp][0], pk[2 * tp + 1][0], false, false);
      auto r1 = __builtin_amdgcn_permlane16_swap(pk[2 * tp][1], pk[2 * tp + 1][1], false, false);
      u32x4 w; w[0] = r0[0]; w[1] = r1[0]; w[2] = r0[1]; w[3] = r1[1];
      const int col = n0 + tp * 32 + ((g4 & 1) ? 16 + (g4 - 1) * 4 : g4 * 4);
      st16c(rs, xo + frag_off<18>(row, col), w);
    }
    if (g4 < 2) { u32x2 w; w[0] = pk[4][0]; w[1] = pk[4][1]; st8c(rs, xo + frag_off<18>(row, n0 + 64 + g4 * 4), w); }
    s += __shfl_xor(s, 16); s += __shfl_xor(s, 32);
    const float mean = s * (1.0f / 72.0f);
    float q = 0.f;
#pragma unroll
    for (int T = 0; T < 5; ++T) {
      if (T < 4 || g4 < 2) {
#pragma unroll
        for (int j = 0; j < 4; ++j) { const float d = v[T][j] - mean; q += d * d; }
      }
    }
    q += __shfl_xor(q, 16); q += __shfl_xor(q, 32);
    if (g4 == 0) { u32x2 w; w[0] = __float_as_uint(mean); w[1] = __float_as_uint(q); st8c(rs, WS_STAT + l * 131072 + (row * 8 + nt) * 8, w); }
    WAIT_V(0);
  }
  signal(c.ws, l * 512 + C_S2 + (mt >> 2));
  STAMP(l * 4 + 1, 4);
}

__device__ __forceinline__ void phase3(const Ctx& c, rsrc_t rs, int l) {
  int tid = threadIdx.x; asm volatile("" : "+v"(tid));
  const int lane = tid & 63, wave = c.wave;
  ldsp lds = c.lds;
  const int xcd = c.bid & 7, idx = c.bid >> 3;
  const int mp = (xcd & 1) * 4 + (idx & 3), ft = (xcd >> 1) * 8 + (idx >> 2);
  const int m0 = mp * 256, f0 = ft * 144;
  const int grp = wave >> 2, wm = wave & 3;
  STAMP(l * 4 + 2, 0);
  if (wave == 1) poll(c.ws, l * 512 + C_S2 + mp, 32u);
  BAR();
  STAMP(l * 4 + 2, 1);
  const char* xb = (const char*)c.ws + WS_X1 + l * XB + (size_t)(mp * 16 + wave * 2) * 18432;
  const char* wb = (const char*)c.ws + WS_WG + (size_t)ft * 18 * 18432 + wave * 1024;
  const unsigned l16 = lane * 16;
  auto piece = [&](int jj, int sl, int i) {
    if (i < 2) glds16c(xb + i * 18432 + jj * 1024, l16, c.lbase + sl * P3_XS + (wave * 2 + i) * 1024);
    else if (i < 4 || wave < 2) glds16(wb + (size_t)jj * 18432 + (i - 2) * 8192, l16, c.lbase + P3_WOFF + sl * P3_WS + (wave + (i - 2) * 8) * 1024);
  };
#pragma unroll
  for (int p = 0; p < 3; ++p) { piece(p, p, 0); piece(p, p, 1); }
  if (grp == 0) {
    const unsigned so = WS_STAT + l * 131072 + (m0 + tid) * 64;
    const f32x4 a = __builtin_bit_cast(f32x4, ld16c(rs, so)), b = __builtin_bit_cast(f32x4, ld16c(rs, so + 16)),
                cq = __builtin_bit_cast(f32x4, ld16c(rs, so + 32)), d = __builtin_bit_cast(f32x4, ld16c(rs, so + 48));
    const float mean = ((a[0] + a[2]) + (b[0] + b[2]) + (cq[0] + cq[2]) + (d[0] + d[2])) * 0.125f;
    float m2 = (a[1] + a[3]) + (b[1] + b[3]) + (cq[1] + cq[3]) + (d[1] + d[3]);
    float e0 = a[0] - mean, e1 = a[2] - mean, e2 = b[0] - mean, e3 = b[2] - mean, e4 = cq[0] - mean, e5 = cq[2] - mean, e6 = d[0] - mean, e7 = d[2] - mean;
    m2 += 72.0f * (((e0 * e0 + e1 * e1) + (e2 * e2 + e3 * e3)) + ((e4 * e4 + e5 * e5) + (e6 * e6 + e7 * e7)));
    const float vb = rb(m2 * (1.0f / 576.0f));
    const float den = rb(__builtin_amdgcn_sqrtf(rb(vb + 1e-5f)));
    *(LAS f32x4*)(lds + P3_STAT + tid * 16) = (f32x4){rb(mean), -1.4426950408889634f * __builtin_amdgcn_rcpf(den), den * den, 0.f};
  }
  WAIT_V(0);
  WAIT_L0();
  BAR();
  STAMP(l * 4 + 2, 2);
  f32x4 acc[4][9];
#pragma unroll
  for (int i = 0; i < 4; ++i)
#pragma unroll
    for (int T = 0; T < 9; ++T) acc[i][T] = (f32x4){0, 0, 0, 0};
  const int abase = wm * 4096 + lane * 16;
  const int wbase = P3_WOFF + grp * 9216 + lane * 16;
  auto rdW = [&](int sl, int T) -> bf16x8 { return lds_b128(lds + sl * P3_WS + wbase + T * 1024); };
  auto rdX = [&](int sl, int i) -> bf16x8 { return lds_b128(lds + sl * P3_XS + abase + i * 1024); };
  bf16x8 xf[2][4], wf[4];
#pragma unroll
  for (int i = 0; i < 4; ++i) xf[0][i] = rdX(0, i);
#pragma unroll
  for (int T = 0; T < 3; ++T) wf[T] = rdW(0, T);
#pragma unroll
  for (int i = 0; i < 4; ++i)
#pragma unroll
    for (int T = 0; T < 9; ++T) { if (T < 8) PINA(acc[i][T]); else PIN(acc[i][T]); }
  NOPS();
  SCHED();
  auto phase = [&](auto JM, auto LAST, int j) {
    constexpr int jm = decltype(JM)::value;
    constexpr bool last = decltype(LAST)::value;
    constexpr int cur = jm & 1, nx = cur ^ 1, sn = (jm + 1) & 3;
#pragma unroll
    for (int T = 0; T < 9; ++T) {
      if (T + 3 < 9) wf[(9 * jm + T + 3) & 3] = rdW(jm, T + 3);
      else if (!last) wf[(9 * jm + T + 3) & 3] = rdW(sn, T + 3 - 9);
      if (!last && T >= 4 && T < 8) xf[nx][T - 4] = rdX(sn, T - 4);
      if (T >= 4 && j < 15) piece(j + 3, (jm + 3) & 3, T - 4);
#pragma unroll
      for (int i = 0; i < 4; ++i) { if (T < 8) MFMA_ACCA(acc[i][T], wf[(9 * jm + T) & 3], xf[cur][i]); else MFMA_ACC(acc[i][T], wf[(9 * jm + T) & 3], xf[cur][i]); }
      if (!last && T == 3) {
        if (j < 16) WAIT_V(4); else WAIT_V(0);
        BAR();
      }
      SCHED();
    }
  };
  typedef std::integral_constant<bool, false> F_t;
#pragma unroll 1
  for (int j = 0; j < 16; j += 4) {
    phase(std::integral_constant<int, 0>{}, F_t{}, j);
    phase(std::integral_constant<int, 1>{}, F_t{}, j + 1);
    phase(std::integral_constant<int, 2>{}, F_t{}, j + 2);
    phase(std::integral_constant<int, 3>{}, F_t{}, j + 3);
  }
  phase(std::integral_constant<int, 0>{}, F_t{}, 16);
  phase(std::integral_constant<int, 1>{}, std::integral_constant<bool, true>{}, 17);
  NOPS();
  SCHED();
  BAR();
  STAMP(l * 4 + 2, 3);
  {
  int tz = threadIdx.x; asm volatile("" : "+v"(tz));
  const int fr = tz & 15, g4 = (tz >> 4) & 3;
  const int cb = grp ? 80 : 0;
  const int cm = grp ? 72 : 64;
  const unsigned ho = WS_H + l * HB;
  f32x4 csg[4], csu[4], csm;
#pragma unroll
  for (int T = 0; T < 4; ++T) {
    csg[T] = *(LAS f32x4*)(lds + P3_CS + (grp * 144 + T * 16 + g4 * 4) * 4);
    csu[T] = *(LAS f32x4*)(lds + P3_CS + (grp * 144 + 64 + T * 16 + g4 * 4) * 4);
  }
  csm = *(LAS f32x4*)(lds + P3_CS + (grp * 144 + 128 + g4 * 4) * 4);
  unsigned md[4];
#pragma unroll
  for (int i = 0; i < 4; ++i) {
    const int r = wm * 64 + i * 16 + fr;
    const f32x4 sc = *(LAS f32x4*)(lds + P3_STAT + r * 16);
    unsigned pk[4][2];
#pragma unroll
    for (int T = 0; T < 4; ++T) {
      const f32x4 tg = acc[i][T] - csg[T] * sc[0];
      const f32x4 tu = acc[i][4 + T] - csu[T] * sc[0];
      const f32x4 ea = tg * sc[1];
      f32x4 e; e[0] = __builtin_amdgcn_exp2f(ea[0]); e[1] = __builtin_amdgcn_exp2f(ea[1]); e[2] = __builtin_amdgcn_exp2f(ea[2]); e[3] = __builtin_amdgcn_exp2f(ea[3]);
      const f32x4 dn = e * sc[2] + sc[2];
      f32x4 rc; rc[0] = __builtin_amdgcn_rcpf(dn[0]); rc[1] = __builtin_amdgcn_rcpf(dn[1]); rc[2] = __builtin_amdgcn_rcpf(dn[2]); rc[3] = __builtin_amdgcn_rcpf(dn[3]);
      const f32x4 hv = (tg * tu) * rc;
      pk[T][0] = pk2(hv[0], hv[1]); pk[T][1] = pk2(hv[2], hv[3]);
    }
#pragma unroll
    for (int tp = 0; tp < 2; ++tp) {
      auto r0 = __builtin_amdgcn_permlane16_swap(pk[2 * tp][0], pk[2 * tp + 1][0], false, false);
      auto r1 = __builtin_amdgcn_permlane16_swap(pk[2 * tp][1], pk[2 * tp + 1][1], false, false);
      u32x4 w; w[0] = r0[0]; w[1] = r1[0]; w[2] = r0[1]; w[3] = r1[1];
      const int col = cb + tp * 32 + ((g4 & 1) ? 16 + (g4 - 1) * 4 : g4 * 4);
      st16c(rs, ho + frag_off<144>(m0 + r, f0 + col), w);
    }
    {
      const f32x4 tm = acc[i][8] - csm * sc[0];
      auto s0 = __builtin_amdgcn_permlane32_swap(__float_as_uint(tm[0]), __float_as_uint(tm[2]), false, false);
      auto s1 = __builtin_amdgcn_permlane32_swap(__float_as_uint(tm[1]), __float_as_uint(tm[3]), false, false);
      const f32x2 tg = {__uint_as_float(s0[0]), __uint_as_float(s1[0])};
      const f32x2 tu = {__uint_as_float(s0[1]), __uint_as_float(s1[1])};
      const f32x2 ea = tg * sc[1];
      f32x2 e; e[0] = __builtin_amdgcn_exp2f(ea[0]); e[1] = __builtin_amdgcn_exp2f(ea[1]);
      const f32x2 dn = e * sc[2] + sc[2];
      f32x2 rc; rc[0] = __builtin_amdgcn_rcpf(dn[0]); rc[1] = __builtin_amdgcn_rcpf(dn[1]);
      const f32x2 hv = (tg * tu) * rc;
      md[i] = pk2(hv[0], hv[1]);
    }
  }
  {
    auto a0 = __builtin_amdgcn_permlane16_swap(md[0], md[1], false, false);
    auto a1 = __builtin_amdgcn_permlane16_swap(md[2], md[3], false, false);
    auto b0 = __builtin_amdgcn_permlane32_swap(a0[0], a1[0], false, false);
    auto b1 = __builtin_amdgcn_permlane32_swap(a0[1], a1[1], false, false);
    u32x4 w; w[0] = b0[0]; w[1] = b0[1]; w[2] = b1[0]; w[3] = b1[1];
    st16c(rs, ho + frag_off<144>(m0 + wm * 64 + g4 * 16 + fr, f0 + cm), w);
  }
  SCHED();
  p4_prefetch_w(c, tz & 63);
  }
  WAIT_V(6);
  signal(c.ws, l * 512 + C_S3 + mp * 4 + (ft >> 3));
  STAMP(l * 4 + 2, 4);
}

__device__ __forceinline__ void phase4(const Ctx& c, rsrc_t rs, int l, u16* out) {
  int tid = threadIdx.x; asm volatile("" : "+v"(tid));
  const int lane = tid & 63, wave = c.wave;
  ldsp lds = c.lds;
  const int xcd = c.bid & 7, idx = c.bid >> 3;
  const int ks = xcd >> 1, m4 = (xcd & 1) * 8 + (idx & 7), n4 = idx >> 3;
  const int m0 = m4 * 128, n0 = n4 * 144;
  const int tile = m4 * 4 + n4;
  const int fr = lane & 15, g4 = lane >> 4;
  const unsigned so_ = WS_SLAB + l * SLABB;
  STAMP(l * 4 + 3, 0);
  if (wave == 7) poll(c.ws, l * 512 + C_S3 + (m4 >> 1) * 4 + ks, 8u);
  BAR();
  STAMP(l * 4 + 3, 1);
  const unsigned l16 = lane * 16;
  if (wave >= 4) {
    const int lw = wave - 4;
    const char* hb = (const char*)c.ws + WS_H + l * HB + (size_t)((m4 * 8 + 2 * lw) * 144 + ks * 36) * 1024;
    const char* wb = (const char*)c.ws + WS_WD + (size_t)(n4 * 144 + ks * 36) * 9216 + lw * 1024;
    auto stageX = [&](int st) {
      const unsigned sl = c.lbase + (st & 3) * P4_ST;
#pragma unroll
      for (int i = 0; i < 4; ++i) glds16c(hb + (size_t)st * 2048 + (i >> 1) * (144 * 1024) + (i & 1) * 1024, l16, sl + lw * 4096 + i * 1024);
    };
    auto stageW = [&](int st) {
      const unsigned sl = c.lbase + (st & 3) * P4_ST;
#pragma unroll
      for (int i = 0; i < 4; ++i) glds16(wb + (size_t)st * 18432 + i * 4096, l16, sl + P4_XS + (lw + 4 * i) * 1024);
      if (lw < 2) glds16(wb + (size_t)st * 18432 + 16384, l16, sl + P4_XS + (lw + 16) * 1024);
    };
#define TOUCH_D 0
    unsigned tsink = 0;
    const unsigned toff = lane < 32 ? WS_H + l * HB + (unsigned)(((m4 * 8 + 2 * n4 + (lane >> 4)) * 144 + ks * 36) * 1024 + (lane & 15) * 128)
                                    : WS_WD + (unsigned)((n4 * 144 + ks * 36) * 9216 + ((idx & 7) * 18 + (lane < 50 ? lane - 32 : 0)) * 128);
    const unsigned tstep = lane < 32 ? 2048u : 18432u;
    auto touch = [&](int st) {
      if (TOUCH_D > 0 && lw == 3 && st < 18) asm volatile("global_load_dword %0, %1, %2" : "+v"(tsink) : "v"(toff + st * tstep), "s"(c.ws) : "memory");
    };
    stageX(0); stageX(1); stageX(2);
#pragma unroll
    for (int t = 3; t < TOUCH_D; ++t) touch(t);
    WAIT_V(0);
    BAR();
#pragma unroll 1
    for (int st = 0; st < 17; ++st) {
      if (st < 16) WAIT_V(8); else WAIT_V(0);
      BAR();
      if (st < 15) { touch(st + TOUCH_D); stageX(st + 3); }
      if (st < 15) { stageW(st + 3); }
    }
    asm volatile("" :: "v"(tsink));
    BAR();
    BAR();
    BAR();
  } else {
    const int wm = wave & 1, kg = wave >> 1;
    WAIT_V(0);
    BAR();
    STAMP(l * 4 + 3, 2);
    f32x4 acc[4][9];
#pragma unroll
    for (int i = 0; i < 4; ++i)
#pragma unroll
      for (int T = 0; T < 9; ++T) acc[i][T] = (f32x4){0, 0, 0, 0};
    const int abase = (wm * 8 + kg) * 1024 + lane * 16;
    const int wbase = P4_XS + kg * 9216 + lane * 16;
    auto rdW = [&](int sl, int T) -> bf16x8 { return lds_b128(lds + sl * P4_ST + wbase + T * 1024); };
    auto rdX = [&](int sl, int i) -> bf16x8 { return lds_b128(lds + sl * P4_ST + abase + i * 2048); };
    bf16x8 xf[2][4], wf[6];
#pragma unroll
    for (int i = 0; i < 4; ++i) xf[0][i] = rdX(0, i);
#pragma unroll
    for (int T = 0; T < 5; ++T) wf[T] = rdW(0, T);
#pragma unroll
    for (int i = 0; i < 4; ++i)
#pragma unroll
      for (int T = 0; T < 9; ++T) { if (T < 8) PINA(acc[i][T]); else PIN(acc[i][T]); }
    NOPS();
    SCHED();
    auto phase = [&](auto JM, auto LAST) {
      constexpr int jm = decltype(JM)::value;
      constexpr bool last = decltype(LAST)::value;
      constexpr int cur = jm & 1, nx = cur ^ 1, sn = (jm + 1) & 3;
#pragma unroll
      for (int T = 0; T < 9; ++T) {
        if (T + 5 < 9) wf[(9 * jm + T + 5) % 6] = rdW(jm, T + 5);
        else if (!last) wf[(9 * jm + T + 5) % 6] = rdW(sn, T + 5 - 9);
        if (!last && T >= 4 && T < 8) xf[nx][T - 4] = rdX(sn, T - 4);
#pragma unroll
        for (int i = 0; i < P4_MI; ++i) MFMA_ACC(acc[i][T], wf[(9 * jm + T) % 6], xf[cur][i]);
        if (!last && T == 3) BAR();
        SCHED();
      }
    };
    typedef std::integral_constant<bool, false> F_t;
#pragma unroll 1
    for (int j = 0; j < 16; j += 4) {
      phase(std::integral_constant<int, 0>{}, F_t{});
      phase(std::integral_constant<int, 1>{}, F_t{});
      phase(std::integral_constant<int, 2>{}, F_t{});
      phase(std::integral_constant<int, 3>{}, F_t{});
    }
    phase(std::integral_constant<int, 0>{}, F_t{});
    phase(std::integral_constant<int, 1>{}, std::integral_constant<bool, true>{});
    NOPS();
    SCHED();
    BAR();
    STAMP(l * 4 + 3, 3);
#pragma unroll
    for (int i = 0; i < 2; ++i)
#pragma unroll
      for (int T = 0; T < 9; ++T)
        *(LAS f32x4*)(lds + P4_EX + ((wave * 18 + i * 9 + T) * 1024 + lane * 16)) = kg ? acc[i][T] : acc[2 + i][T];
    WAIT_L0();
    BAR();
    const int qd = wm * 2 + kg;
    f32x4 fin[2][9];
#pragma unroll
    for (int i = 0; i < 2; ++i)
#pragma unroll
      for (int T = 0; T < 9; ++T)
        fin[i][T] = (kg ? acc[2 + i][T] : acc[i][T]) + *(LAS f32x4*)(lds + P4_EX + (((wave ^ 2) * 18 + i * 9 + T) * 1024 + lane * 16));
    WAIT_L0();
    BAR();
    STAMP(l * 4 + 3, 4);
    if (qd != ks) {
      const unsigned so = so_ + (unsigned)(((tile * 4 + qd) * 4 + ks) * 18432 + lane * 16);
#pragma unroll
      for (int i = 0; i < 2; ++i)
#pragma unroll
        for (int T = 0; T < 9; ++T) st16c(rs, so + (i * 9 + T) * 1024, __builtin_bit_cast(u32x4, fin[i][T]));
      WAIT_V(0);
      if (lane == 0) __hip_atomic_fetch_add((GAS unsigned*)(c.ws + WS_CNT + (l * 512 + C_S4 + tile * 4 + qd) * 256), 1u, __ATOMIC_RELAXED, __HIP_MEMORY_SCOPE_AGENT);
    } else {
#pragma unroll
      for (int i = 0; i < 2; ++i)
#pragma unroll
        for (int T = 0; T < 9; ++T) *(LAS f32x4*)(lds + P4_OWN + (i * 9 + T) * 1024 + lane * 16) = fin[i][T];
      WAIT_L0();
    }
  }
  STAMP(l * 4 + 3, 5);
  if (wave == 4) poll(c.ws, l * 512 + C_S4 + tile * 4 + ks, 3u);
  u32x2 res[2][2];
#pragma unroll
  for (int uu = 0; uu < 2; ++uu) {
    const int u = wave + 8 * uu;
    if (u < 10) {
      const int i = u / 5, tp = u % 5;
#pragma unroll
      for (int t = 0; t < 2; ++t) {
        const int T = (tp * 2 + t) < 9 ? tp * 2 + t : 8;
        res[uu][t] = ld8c(rs, WS_X1 + l * XB + frag_off<18>(m0 + ks * 32 + i * 16 + fr, n0 + T * 16 + g4 * 4));
      }
    }
  }
  BAR();
  STAMP(l * 4 + 3, 6);
#pragma unroll
  for (int uu = 0; uu < 2; ++uu) {
    const int u = wave + 8 * uu;
    if (u < 10) {
      const int i = u / 5, tp = u % 5;
      f32x4 p[2][3], own[2];
#pragma unroll
      for (int t = 0; t < 2; ++t) {
        const int f = i * 9 + ((tp * 2 + t) < 9 ? tp * 2 + t : 8);
#pragma unroll
        for (int v = 0; v < 3; ++v) {
          const int src = v + (v >= ks ? 1 : 0);
          p[t][v] = __builtin_bit_cast(f32x4, ld16c(rs, so_ + (unsigned)(((tile * 4 + ks) * 4 + src) * 18432 + f * 1024 + lane * 16)));
        }
        own[t] = *(LAS f32x4*)(lds + P4_OWN + f * 1024 + lane * 16);
      }
      unsigned pk[2][2];
#pragma unroll
      for (int t = 0; t < 2; ++t) {
        const f32x4 a0 = ks == 0 ? own[t] : p[t][0], a1 = ks == 1 ? own[t] : (ks < 1 ? p[t][0] : p[t][1]),
                    a2 = ks == 2 ? own[t] : (ks < 2 ? p[t][1] : p[t][2]), a3 = ks == 3 ? own[t] : p[t][2];
        const f32x4 sm = (a0 + a1) + (a2 + a3);
        pk[t][0] = pk2(lo16(res[uu][t][0]) + rb(sm[0]), hi16(res[uu][t][0]) + rb(sm[1]));
        pk[t][1] = pk2(lo16(res[uu][t][1]) + rb(sm[2]), hi16(res[uu][t][1]) + rb(sm[3]));
      }
      auto r0 = __builtin_amdgcn_permlane16_swap(pk[0][0], pk[1][0], false, false);
      auto r1 = __builtin_amdgcn_permlane16_swap(pk[0][1], pk[1][1], false, false);
      u32x4 w; w[0] = r0[0]; w[1] = r1[0]; w[2] = r0[1]; w[3] = r1[1];
      const int row = m0 + ks * 32 + i * 16 + fr;
      const int col = n0 + tp * 32 + ((g4 & 1) ? 16 + (g4 - 1) * 4 : g4 * 4);
      if (tp < 4 || !(g4 & 1)) {
        if (l + 1 < NLAYERS) st16c(rs, WS_X2 + frag_off<18>(row, col), w);
        else *(u32x4*)((char*)out + (size_t)row * 1152 + col * 2) = w;
      }
    }
  }
  if (l + 1 < NLAYERS) {
    SCHED();
    p1_prefetch_w(c, lane);
    WAIT_V(9);
    signal(c.ws, C_S5 + m4 * 4 + ks);
  }
  STAMP(l * 4 + 3, 7);
}

__global__ __launch_bounds__(512, 2) void k_mega(unsigned char* __restrict__ ws, u16* __restrict__ out) {
  extern __shared__ __attribute__((aligned(16))) unsigned char smem_[];
  Ctx c;
  c.ws = ws; c.lds = (ldsp)smem_; c.lbase = (unsigned)(size_t)c.lds;
  c.wave = __builtin_amdgcn_readfirstlane(threadIdx.x >> 6); c.bid = blockIdx.x;
  const rsrc_t rs = __builtin_amdgcn_make_buffer_rsrc(ws, 0, WS_END, 0x00020000);
  p1_prefetch_w(c, threadIdx.x & 63);
#define OPAQUE() asm volatile("" : "+s"(c.ws), "+s"(c.wave), "+s"(c.bid), "+s"(c.lbase), "+s"(l))
#pragma unroll 1
  for (int l = 0; l < NLAYERS; ++l) {
    OPAQUE(); phase1(c, rs, l);
    OPAQUE(); phase2(c, rs, l);
    OPAQUE(); phase3(c, rs, l);
    OPAQUE(); phase4(c, rs, l, out);
  }
}

extern "C" void kernel_launch(void* const* d_in, const int* in_sizes, int n_in, void* d_out, int out_size, void* d_ws, size_t ws_size,
                              hipStream_t stream) {
  (void)hipFuncSetAttribute((const void*)k_prep, hipFuncAttributeMaxDynamicSharedMemorySize, 8 * PREP_WLDS);
  (void)hipFuncSetAttribute((const void*)k_mega, hipFuncAttributeMaxDynamicSharedMemorySize, MK_LDS);
  k_prep<<<256, 512, 8 * PREP_WLDS, stream>>>((const u16*)d_in[0], (const u16*)d_in[1], (const u16*)d_in[2], (const u16*)d_in[4],
                                              (const u16*)d_in[5], (const u16*)d_in[6], (unsigned char*)d_ws);
  k_mega<<<256, 512, MK_LDS, stream>>>((unsigned char*)d_ws, (u16*)d_out);
}
```

```cpp
#define NLAYERS 2
#define PW0 2
#define TWN 1
#include <hip/hip_runtime.h>
#include <hip/hip_bf16.h>
#include <type_traits>

typedef unsigned short u16;
typedef short bf16x8 __attribute__((ext_vector_type(8)));
typedef short bf16x4 __attribute__((ext_vector_type(4)));
typedef float f32x4 __attribute__((ext_vector_type(4)));
typedef float f32x2 __attribute__((ext_vector_type(2)));
typedef unsigned u32x4 __attribute__((ext_vector_type(4)));
typedef unsigned u32x2 __attribute__((ext_vector_type(2)));
typedef __bf16 bf2_t __attribute__((ext_vector_type(2)));
#define LAS __attribute__((address_space(3)))
#define GAS __attribute__((address_space(1)))
typedef LAS unsigned char* ldsp;

#define WAIT_V(n) asm volatile("s_waitcnt vmcnt(" #n ")" ::: "memory")
#define WAIT_L0() asm volatile("s_waitcnt lgkmcnt(0)" ::: "memory")
#define BAR() do { asm volatile("" ::: "memory"); __builtin_amdgcn_s_barrier(); asm volatile("" ::: "memory"); } while (0)
#define SCHED() __builtin_amdgcn_sched_barrier(0)
#define MFMA(a, b, c) __builtin_amdgcn_mfma_f32_16x16x32_bf16(a, b, c, 0, 0, 0)
#define MFMA_ACC(c, a, b) asm volatile("v_mfma_f32_16x16x32_bf16 %0, %1, %2, %0" : "+v"(c) : "v"(a), "v"(b))
#define NOPS() asm volatile("s_nop 7\n\ts_nop 7" ::: "memory")
#define PIN(x) asm volatile("" : "+v"(x))

#define GNOP "3"
#define GSC " sc1"
__device__ __forceinline__ void glds16(const void* sbase, unsigned voff, unsigned ldst) {
  asm volatile("s_mov_b32 m0, %2\n\ts_nop " GNOP "\n\tglobal_load_lds_dwordx4 %0, %1" :: "v"(voff), "s"(sbase), "s"(ldst) : "memory");
}
__device__ __forceinline__ void glds16c(const void* sbase, unsigned voff, unsigned ldst) {
  asm volatile("s_mov_b32 m0, %2\n\ts_nop " GNOP "\n\tglobal_load_lds_dwordx4 %0, %1" GSC :: "v"(voff), "s"(sbase), "s"(ldst) : "memory");
}
__device__ __forceinline__ bf16x8 lds_b128(ldsp p) { return *(const LAS bf16x8*)p; }
__device__ __forceinline__ bf16x4 lds_tr(ldsp p) { return __builtin_amdgcn_ds_read_tr16_b64_v4i16((LAS bf16x4*)p); }
__device__ __forceinline__ bf16x8 cat8(bf16x4 a, bf16x4 b) { return __builtin_shufflevector(a, b, 0, 1, 2, 3, 4, 5, 6, 7); }
__device__ __forceinline__ unsigned pk2(float lo, float hi) {
  bf2_t v; v[0] = (__bf16)lo; v[1] = (__bf16)hi; return __builtin_bit_cast(unsigned, v);
}
__device__ __forceinline__ float dot2(unsigned a, unsigned b, float c) {
  return __builtin_amdgcn_fdot2_f32_bf16(__builtin_bit_cast(bf2_t, a), __builtin_bit_cast(bf2_t, b), c, false);
}
__device__ __forceinline__ float swap1(float x) {
  return __int_as_float(__builtin_amdgcn_mov_dpp(__float_as_int(x), 0xB1, 0xf, 0xf, true));
}
typedef _Float16 h2_t __attribute__((ext_vector_type(2)));
__device__ __forceinline__ unsigned pkh(float lo, float hi) { h2_t v; v[0] = (_Float16)lo; v[1] = (_Float16)hi; return __builtin_bit_cast(unsigned, v); }
__device__ __forceinline__ float h2f(unsigned h) { return (float)__builtin_bit_cast(_Float16, (unsigned short)h); }
__device__ __forceinline__ float sum_g4(float x) {
  auto a = __builtin_amdgcn_permlane16_swap(__float_as_uint(x), __float_as_uint(x), false, false);
  x = __uint_as_float(a[0]) + __uint_as_float(a[1]);
  auto b = __builtin_amdgcn_permlane32_swap(__float_as_uint(x), __float_as_uint(x), false, false);
  return __uint_as_float(b[0]) + __uint_as_float(b[1]);
}
__device__ __forceinline__ float amax3(float m, float a, float b) { asm("v_max3_f32 %0, %0, |%1|, |%2|" : "+v"(m) : "v"(a), "v"(b)); return m; }
__device__ __forceinline__ float rb(float x) { return (float)(__bf16)x; }
__device__ __forceinline__ float lo16(unsigned u) { return __uint_as_float(u << 16); }
__device__ __forceinline__ float hi16(unsigned u) { return __uint_as_float(u & 0xffff0000u); }
template <int NKB> __device__ __forceinline__ unsigned frag_off(int row, int col) {
  return (unsigned)(((row >> 4) * NKB + (col >> 5)) * 1024 + ((col >> 3) & 3) * 256 + (row & 15) * 16 + ((col >> 2) & 1) * 8);
}

constexpr unsigned XB = 2048u * 576u * 2u;
constexpr unsigned HB = 2048u * 4608u * 2u;
constexpr unsigned WS_CNT = 0;
constexpr unsigned WS_X0 = WS_CNT + 4352u * 256u;
constexpr unsigned WS_X2 = WS_X0 + XB;
constexpr unsigned WS_O = WS_X2 + XB;
constexpr unsigned WS_X1 = WS_O + 2 * XB;
constexpr unsigned WS_H = WS_X1 + 2 * XB;
constexpr unsigned SLABB = 64u * 16u * 9472u;
constexpr unsigned WS_SLAB = WS_H + 2 * HB;
constexpr unsigned WS_STAT = WS_SLAB + 2 * SLABB;
constexpr unsigned WS_WQ = WS_STAT + 2 * 2048u * 64u;
constexpr unsigned WO_ST = 9472u, WO_B = 86016u;
constexpr unsigned WS_WO = WS_WQ + 8u * 18u * 14336u;
constexpr unsigned WS_WD = WS_WO + 8u * WO_B;
constexpr unsigned WS_XC = WS_WD + 4u * 144u * 9216u;
constexpr unsigned WS_CSQ = WS_XC + 2 * XB;
constexpr unsigned WS_END = WS_CSQ + 18u * 8u * 224u * 4u;
#define R1 8
#define R2 32
#define R3 8
#define R5 16
constexpr int C_L = 1536, C_S1 = 0, C_S2 = 256, C_S3 = 512, C_S3B = 768, C_S3C = 1024, C_S4 = 1280, C_S5 = 3072, C_WD = 4096;

typedef __amdgpu_buffer_rsrc_t rsrc_t;
__device__ __forceinline__ void st16c(rsrc_t rs, unsigned off, u32x4 v) { __builtin_amdgcn_raw_buffer_store_b128(v, rs, off, 0, 16); }
__device__ __forceinline__ void stf(rsrc_t rs, unsigned off, bf16x8 v) { st16c(rs, off, __builtin_bit_cast(u32x4, v)); }
constexpr int PREP_WLDS = 18432 + 64;
#define PREP_NW 4
extern "C" __global__ __launch_bounds__(64 * PREP_NW) void k_prep(const u16* __restrict__ x, const u16* __restrict__ wqkv, const u16* __restrict__ wo,
                                                                unsigned char* __restrict__ ws) {
  extern __shared__ __attribute__((aligned(16))) unsigned char smem_[];
  const int tid = threadIdx.x, lane = tid & 63, wave = __builtin_amdgcn_readfirstlane(tid >> 6);
  const rsrc_t rs = __builtin_amdgcn_make_buffer_rsrc(ws, 0, WS_END, 0x00020000);
  *(u32x4*)(ws + WS_CNT + blockIdx.x * 4096 + tid * 16) = (u32x4){0u, 0u, 0u, 0u};
  if (tid < 16) *(u32x4*)(ws + WS_CNT + 4096 * 256 + blockIdx.x * 256 + tid * 16) = (u32x4){0u, 0u, 0u, 0u};
  unsigned sink = 0;
  {
    unsigned long long pc; unsigned off, len;
    asm volatile("s_getpc_b64 %0\n.Lpk_pc:\n\ts_mov_b32 %1, k_mega-.Lpk_pc\n\ts_mov_b32 %2, mk_end-k_mega" : "=s"(pc), "=s"(off), "=s"(len));
    unsigned o = (unsigned)((blockIdx.x * 2 + (lane & 1)) * 128);
    o = o + 4 <= len ? o : 0u;
    if (wave == 0) asm volatile("global_load_dword %0, %1, %2" : "+v"(sink) : "v"(o), "s"(pc + off) : "memory");
  }
  const int g4 = lane >> 4, p4 = lane & 3, fr = lane & 15;
  auto issue = [&](int u, unsigned lb) {
    if (u < 1008) {
      const int v = u - 864, hg = v & 7, j = v >> 3;
#pragma unroll
      for (int i = 0; i < 14; ++i) {
        int b = i * 1024 + lane * 16; b = b < 13824 ? b : 13824 - 16;
        const int pr = b / 864, rem = b % 864, t = rem / 432, c = (rem % 432) >> 4;
        const int k = ((pr >> 2) & 3) * 8 + t * 4 + (pr & 3);
        const int seg = c / 9, cc = c % 9;
        glds16(wqkv, (unsigned)((j * 32 + k) * 3456 + (seg * 576 + hg * 72 + cc * 8) * 2), lb + i * 1024);
      }
    } else if (u < 1080) {
      const int v = u - 1008, nt = v & 7, st = v >> 3;
#pragma unroll
      for (int i = 0; i < 9; ++i) {
        const int b = i * 1024 + lane * 16;
        const int pr = b / 288, rem = b % 288, t = rem / 144, c = (rem % 144) >> 4;
        const int k = (pr >> 4) * 32 + ((pr >> 2) & 3) * 8 + t * 4 + (pr & 3);
        glds16(wo, (unsigned)((st * 64 + k) * 1152 + nt * 144 + c * 16), lb + i * 1024);
      }
    } else {
      const int m16 = u - 1080;
#pragma unroll
      for (int kb = 0; kb < 18; ++kb) glds16((const char*)x + kb * 64, (unsigned)((m16 * 16 + fr) * 1152 + g4 * 16), lb + kb * 1024);
    }
  };
  auto finish = [&](int u, ldsp lds) {
    const bf16x8 ones = {0x3f80, 0x3f80, 0x3f80, 0x3f80, 0x3f80, 0x3f80, 0x3f80, 0x3f80};
    const f32x4 z4 = {0.f, 0.f, 0.f, 0.f};
    if (u < 1008) {
      const int v = u - 864, hg = v & 7, j = v >> 3;
      const int wb = (lane >> 2) * 864 + p4 * 8;
      const unsigned dst = WS_WQ + (unsigned)((hg * 18 + j) * 14336 + lane * 16);
      float* cs = (float*)(ws + WS_CSQ) + (size_t)(j * 8 + hg) * 224 + g4 * 4;
#pragma unroll
      for (int T = 0; T < 14; ++T) {
        const bf16x8 f = cat8(lds_tr(lds + wb + T * 32), lds_tr(lds + wb + 432 + T * 32));
        stf(rs, dst + T * 1024, f);
        const f32x4 s = MFMA(f, ones, z4);
        if (fr == 0) *(f32x4*)(cs + T * 16) = s;
      }
    } else if (u < 1080) {
      const int v = u - 1008, nt = v & 7, st = v >> 3;
      const int wb = (lane >> 2) * 288 + p4 * 8;
      const unsigned dst = WS_WO + (unsigned)(nt * WO_B + st * WO_ST + lane * 16);
#pragma unroll
      for (int kk = 0; kk < 2; ++kk)
#pragma unroll
        for (int T = 0; T < 4; ++T)
          stf(rs, dst + (kk * 4 + T) * 1024, cat8(lds_tr(lds + wb + kk * 4608 + T * 32), lds_tr(lds + wb + kk * 4608 + 144 + T * 32)));
      {
        const int o = (lane >> 2) * 288 + (p4 & 1) * 8 + (p4 >> 1) * 4608 + 128;
        stf(rs, dst + 8 * 1024, cat8(lds_tr(lds + o), lds_tr(lds + o + 144)));
      }
    } else {
      const int m16 = u - 1080;
#pragma unroll
      for (int kb = 0; kb < 18; ++kb) stf(rs, WS_X0 + (unsigned)((m16 * 18 + kb) * 1024 + lane * 16), lds_b128(lds + kb * 1024 + lane * 16));
    }
  };
  const int bb = (blockIdx.x & 7) * 32 + (blockIdx.x >> 3);
  ldsp lds0 = (ldsp)smem_ + (wave & 1) * PREP_WLDS;
  if (wave < 2) {
    const int u = 864 + bb + 256 * wave;
    if (u < 1208) {
      issue(u, (unsigned)(size_t)lds0);
      WAIT_V(0);
      finish(u, lds0);
    }
  } else if (wave == 2 && bb >= 112) {
    const int k = (bb - 112) * 4 + g4;
    const char* p = (const char*)wo + (size_t)k * 1152 + fr * 8;
    u32x2 v[9];
#pragma unroll
    for (int i = 0; i < 9; ++i) v[i] = *(const u32x2*)(p + i * 128);
    float sm = 0.f;
#pragma unroll
    for (int i = 0; i < 9; ++i) sm += (lo16(v[i][0]) + hi16(v[i][0])) + (lo16(v[i][1]) + hi16(v[i][1]));
#pragma unroll
    for (int o = 8; o > 0; o >>= 1) sm += __shfl_xor(sm, o);
    const float m = sm * (1.0f / 576.0f), hi = rb(m), lo = m - hi;
    *(u16*)(ws + WS_WO + (fr >> 1) * WO_B + (k >> 6) * WO_ST + 9216 + ((k >> 5) & 1) * 128 + (fr & 1) * 64 + (k & 31) * 2) = (u16)(pk2((fr & 1) ? lo : hi, 0.f) & 0xffffu);
  }
  asm volatile("" :: "v"(sink));
}

constexpr int MK_LDS = 163840;
__device__ __forceinline__ void st8c(rsrc_t rs, unsigned off, u32x2 v) { __builtin_amdgcn_raw_buffer_store_b64(v, rs, off, 0, 16); }
__device__ __forceinline__ u32x4 ld16c(rsrc_t rs, unsigned off) { return __builtin_amdgcn_raw_buffer_load_b128(rs, off, 0, 16); }
__device__ __forceinline__ u32x2 ld8c(rsrc_t rs, unsigned off) { return __builtin_amdgcn_raw_buffer_load_b64(rs, off, 0, 16); }
__device__ __forceinline__ unsigned flag_off(int t, int line, int slot) {
  unsigned o = WS_CNT + (unsigned)((line + t) * 256 + slot * 4);
  PIN(o);
  return o;
}
template <int R> __device__ __forceinline__ void flag_at(unsigned char* ws, int t, unsigned off) {
  if (t < R) __hip_atomic_store((GAS unsigned*)(ws + off), 1u, __ATOMIC_RELAXED, __HIP_MEMORY_SCOPE_AGENT);
}
template <int R> __device__ __forceinline__ void flag(unsigned char* ws, int t, int line, int slot) { flag_at<R>(ws, t, flag_off(t, line, slot)); }
__device__ __forceinline__ void lds_spin(unsigned addr, unsigned want) {
  unsigned t, u;
  asm volatile("1:\n\tds_read_b32 %0, %2\n\ts_waitcnt lgkmcnt(0)\n\tv_readfirstlane_b32 %1, %0\n\ts_cmp_ge_u32 %1, %3\n\ts_cbranch_scc1 2f\n\ts_sleep 1\n\ts_branch 1b\n2:"
               : "=&v"(t), "=&s"(u) : "v"(addr), "s"(want) : "memory", "scc");
}
__device__ __forceinline__ bool last_wave(ldsp p, int lane) {
  unsigned old = 0;
  if (lane == 0) old = __hip_atomic_fetch_add((LAS unsigned*)p, 1u, __ATOMIC_RELAXED, __HIP_MEMORY_SCOPE_WORKGROUP);
  return __builtin_amdgcn_readfirstlane(old) == 3u;
}
template <int R> __device__ __forceinline__ void publish(unsigned char* ws, int line, int slot) {
  const unsigned o = flag_off(threadIdx.x, line, slot);
  WAIT_V(0);
  BAR();
  flag_at<R>(ws, threadIdx.x, o);
}
#define PSLEEP 2
__device__ __forceinline__ void poll(unsigned char* ws, int line, int slot) {
  unsigned spins = 0;
  while (__builtin_amdgcn_ballot_w64(__hip_atomic_load((GAS unsigned*)(ws + WS_CNT + line * 256 + slot * 4), __ATOMIC_RELAXED, __HIP_MEMORY_SCOPE_AGENT) == 0u)) {
    __builtin_amdgcn_s_sleep(PSLEEP);
    if (++spins > (1u << 21)) break;
  }
}

constexpr int P1_STAT = 159744, P1_CS = 161792;
constexpr int P1_QROW = 592, P1_QOFF = 86016, P1_OOFF = P1_QOFF + 80 * P1_QROW;
constexpr int P2_SCR = 86016, L_MEAN = 160768;
constexpr int P3_XS = 16384, P3_WS = 18464, P3_UOFF = 9232, P3_WOFF = 4 * P3_XS, P3_STAT = P3_WOFF + 4 * P3_WS, P3_CNT = P3_STAT + 4096;
constexpr int P4_XS = 16384, P4_ST = P4_XS + 18432, P4_EX = 2 * P4_ST, P4_OWN = P4_EX + 73728;

struct Ctx { unsigned char* ws; const u16* wg; const u16* wu; const u16* wd; ldsp lds; unsigned lbase; int wave; int bid; };

template <int N> __device__ __forceinline__ void p2_prefetch_w(const Ctx& c, int lane, int first, int stride) {
  const int xcd = c.bid & 7, idx = c.bid >> 3, nt = (xcd & 1) * 4 + (idx >> 3);
  const char* wb = (const char*)c.ws + WS_WO + (size_t)nt * WO_B;
#pragma unroll
  for (int i = 0; i < N; ++i) {
    const int pc = first + stride * i;
    if (pc < 84) glds16(wb + pc * 1024, lane * 16, c.lbase + pc * 1024);
  }
}
__device__ __forceinline__ unsigned gu_off(int p, int lane, int ft) {
  const int b = p * 1024 + lane * 16;
  const int rho = b / 288, cc = (b % 288) >> 4;
  const int k = (rho & 0x13) | ((rho & 4) << 1) | ((rho & 8) >> 1);
  return (unsigned)(k * 9216 + ft * 288 + cc * 16);
}
__device__ __forceinline__ void p4_prefetch_w(const Ctx& c, int lane) {
  const int xcd = c.bid & 7, idx = c.bid >> 3, ks = xcd >> 1, n4 = idx >> 3;
  const char* wb = (const char*)c.ws + WS_WD + (size_t)(n4 * 144 + ks * 36) * 9216;
#pragma unroll
  for (int i = 0; i < 14; ++i) {
    const int pc = c.wave + 4 * i;
    if (pc < 54) glds16c(wb + pc * 1024, lane * 16, c.lbase + (pc / 18) * P4_ST + P4_XS + (pc % 18) * 1024);
  }
}
__device__ __forceinline__ void phase1(const Ctx& c, rsrc_t rs, int l) {
  int tid = threadIdx.x; asm volatile("" : "+v"(tid));
  const int lane = tid & 63, wave = c.wave;
  ldsp lds = c.lds;
  const int xcd = c.bid & 7, idx = c.bid >> 3;
  const int mt = (xcd >> 1) * 8 + (idx & 7), hg = (xcd & 1) * 4 + (idx >> 3);
  const int m0 = mt * 64;
  const char* xb = (const char*)c.ws + (l ? WS_X2 : WS_X0);
#define P1_NP 5
  const int fr = lane & 15, g4 = lane >> 4;
  const unsigned l16 = lane * 16;
  auto xoff = [&](int pp) -> unsigned {
    const int i = pp % 5, am = mt * 4 - 1 + i < 0 ? 0 : mt * 4 - 1 + i;
    return (unsigned)((am * 18 + pp / 5) * 1024) + l16;
  };
  auto xpiece = [&](int ch, int pp) { glds16c(xb + ch * 6144, xoff(pp), c.lbase + (ch * 30 + pp) * 1024); };
  auto rdX = [&](int j, int i) -> bf16x8 { return lds_b128(lds + l16 + (j * 5 + i) * 1024); };
  float cv[18];
  unsigned co = WS_CSQ + (unsigned)(hg * 224 + (tid < 224 ? tid : 0)) * 4;
  const unsigned st = 8 * 224 * 4;
  PIN(co);
  auto ldCS = [&]() {
#pragma unroll
    for (int j = 0; j < 18; ++j) asm volatile("global_load_dword %0, %1, %2" : "=&v"(cv[j]) : "v"(co + j * st), "s"(c.ws) : "memory");
  };
  const int u = c.bid * 8 + wave;
  unsigned wdo[3];
  auto prepWD = [&]() {
    const bool on = u < 1728 && l == 0;
    const int uu = on ? u : 0;
    const int kb = uu / 12, cg = uu % 12, ksl = kb / 36, r = (kb % 36) * 32;
    const bool mix = (r >= 512 && r < 576) || r >= 1088;
    const int f0 = ksl * 1152 + (r < 512 ? ((r >> 5) & 7) * 144 + 80 + (r >> 8) * 32 : r < 576 ? ((r - 512) >> 3) * 144 + 72
                                 : r < 1088 ? ((r - 576) >> 6) * 144 + (r & 63) : ((r - 1088) >> 3) * 144 + 64);
    const int hs = mix ? 144 : 8;
#pragma unroll
    for (int i = 0; i < 3; ++i) {
      const int id = i * 64 + lane, rr = id / 6;
      wdo[i] = on ? (unsigned)((f0 + ((rr >> 2) & 3) * hs + (rr >> 4) * 4 + (rr & 3)) * 1152 + cg * 96 + (id % 6) * 16) : l16;
      PIN(wdo[i]);
    }
  };
  auto ldWD = [&]() {
#pragma unroll
    for (int i = 0; i < 3; ++i) glds16(c.wd, wdo[i], c.lbase + P1_OOFF + wave * 3072 + i * 1024);
  };
  auto colsum = [&]() {
    WAIT_V(3);
    float s = 0.f;
#pragma unroll
    for (int j = 0; j < 18; ++j) { PIN(cv[j]); s += cv[j]; }
    if (tid < 224) *(LAS float*)(lds + P1_CS + tid * 4) = -s;
  };
  f32x4 acc[5][2];
  bf16x8 xf[2][5];
  unsigned sink = 0;
  if (wave < 7) {
    const char* wb = (const char*)c.ws + WS_WQ + (size_t)hg * 18 * 14336 + wave * 2048;
    bf16x8 w[18][2];
    auto ldW = [&](int j) {
      asm volatile("global_load_dwordx4 %0, %2, %3\n\tglobal_load_dwordx4 %1, %2, %3 offset:1024" : "=&v"(w[j][0]), "=&v"(w[j][1]) : "v"(l16), "s"(wb + j * 14336) : "memory");
    };
#pragma unroll
    for (int j = 0; j < P1_NP; ++j) ldW(j);
    unsigned xo[3];
#pragma unroll
    for (int i = 0; i < 3; ++i) { xo[i] = xoff(wave + 7 * i); PIN(xo[i]); }
    if (l) {
      const unsigned fo = flag_off(tid, C_S5 + (((xcd & 1) * 8 + (idx & 7)) * 4 + (xcd >> 1)) * R5, idx >> 3);
      asm volatile("s_waitcnt vmcnt(%0)" :: "n"(2 * P1_NP) : "memory");
      BAR();
      flag_at<R5>(c.ws, tid, fo);
      BAR();
    }
#pragma unroll
    for (int ch = 0; ch < 3; ++ch)
#pragma unroll
      for (int i = 0; i < 3; ++i) glds16c(xb + ch * 6144, xo[i], c.lbase + (ch * 30 + wave + 7 * i) * 1024);
#pragma unroll
    for (int i = 0; i < 5; ++i) { acc[i][0] = (f32x4){0, 0, 0, 0}; acc[i][1] = (f32x4){0, 0, 0, 0}; PIN(acc[i][0]); PIN(acc[i][1]); }
    prepWD();
    WAIT_V(6);
    BAR();
#pragma unroll
    for (int i = 0; i < 5; ++i) xf[0][i] = rdX(0, i);
    NOPS();
    SCHED();
#pragma unroll
    for (int j = 0; j < 18; ++j) {
      const int cur = j & 1, nx = cur ^ 1;
      if (j >= P1_NP) asm volatile("s_waitcnt vmcnt(%0)" :: "n"(2 * ((j + P1_NP - 1 < 17 ? j + P1_NP - 1 : 17) - j) + (j > 18 - P1_NP ? 21 : 0)) : "memory");
      else if (j == 5) WAIT_V(13);
      if (j == 5 || j == 11) BAR();
      const bool pf = j + 1 < 18;
      if (pf) { xf[nx][0] = rdX(j + 1, 0); xf[nx][1] = rdX(j + 1, 1); }
      MFMA_ACC(acc[0][0], w[j][0], xf[cur][0]); MFMA_ACC(acc[0][1], w[j][1], xf[cur][0]);
      SCHED();
      if (pf) { xf[nx][2] = rdX(j + 1, 2); xf[nx][3] = rdX(j + 1, 3); }
      MFMA_ACC(acc[1][0], w[j][0], xf[cur][1]); MFMA_ACC(acc[1][1], w[j][1], xf[cur][1]);
      SCHED();
      if (j + P1_NP < 18) ldW(j + P1_NP);
      if (j + P1_NP == 18) { ldCS(); ldWD(); }
      if (pf) { xf[nx][4] = rdX(j + 1, 4); }
      MFMA_ACC(acc[2][0], w[j][0], xf[cur][2]); MFMA_ACC(acc[2][1], w[j][1], xf[cur][2]);
      MFMA_ACC(acc[3][0], w[j][0], xf[cur][3]); MFMA_ACC(acc[3][1], w[j][1], xf[cur][3]);
      MFMA_ACC(acc[4][0], w[j][0], xf[cur][4]); MFMA_ACC(acc[4][1], w[j][1], xf[cur][4]);
      SCHED();
    }
    NOPS();
    SCHED();
    colsum();
  } else {
    if (l) {
      WAIT_V(0);
      BAR();
      const int q = 2 * mt - 1 + ((lane >> 2) & 3);
      poll(c.ws, C_S5 + (q < 0 || ((lane >> 2) & 3) == 3 ? 2 * mt : q) * R5 + (hg + (q >= 0 && q < 2 * mt ? 8 : 0)) % R5, lane & 3);
      BAR();
    }
#pragma unroll
    for (int ch = 0; ch < 2; ++ch)
#pragma unroll
      for (int i = 0; i < 9; ++i) xpiece(ch, 21 + i);
    bf16x8 ones = {0x3f80, 0x3f80, 0x3f80, 0x3f80, 0x3f80, 0x3f80, 0x3f80, 0x3f80};
    PIN(ones);
#pragma unroll
    for (int i = 0; i < 5; ++i) { acc[i][0] = (f32x4){0, 0, 0, 0}; acc[i][1] = (f32x4){0, 0, 0, 0}; PIN(acc[i][0]); PIN(acc[i][1]); }
    prepWD();
    asm volatile("s_setprio 3");
    WAIT_V(9);
    BAR();
#pragma unroll
    for (int i = 0; i < 5; ++i) xf[0][i] = rdX(0, i);
    NOPS();
    SCHED();
#pragma unroll
    for (int j = 0; j < 18; ++j) {
      const int cur = j & 1, nx = cur ^ 1;
      if (j == 5) { WAIT_V(14); BAR(); }
      if (j == 11) { asm volatile("s_waitcnt vmcnt(%0)" :: "n"(1 + 4 * (TWN < 3 ? TWN : 3)) : "memory"); BAR(); }
      if (j + 1 < 18) {
#pragma unroll
        for (int i = 0; i < 5; ++i) xf[nx][i] = rdX(j + 1, i);
      }
#pragma unroll
      for (int i = 0; i < 5; ++i) { MFMA_ACC(acc[i][0], ones, xf[cur][i]); MFMA_ACC(acc[i][1], xf[cur][i], xf[cur][i]); }
      if (j < 3) {
#pragma unroll
        for (int i = 0; i < 3; ++i) xpiece(2, 21 + j * 3 + i);
      }
      if (j == 2) {
        unsigned long long pc; unsigned off, len;
        asm volatile("s_getpc_b64 %0\n.Lmk_pc:\n\ts_mov_b32 %1, .Lmk_pc-k_mega\n\ts_mov_b32 %2, mk_end-k_mega" : "=s"(pc), "=s"(off), "=s"(len));
        unsigned o = (unsigned)((lane * 32 + idx) * 128);
        o = o + 4 <= len ? o : 0u;
        asm volatile("global_load_dword %0, %1, %2" : "+v"(sink) : "v"(o), "s"(pc - off) : "memory");
      }
      if (j % 3 == 0 && j > 0 && j / 3 <= TWN) {
        const int ft = (xcd >> 1) * 8 + (idx >> 2);
#pragma unroll
        for (int i = 0; i < 4; ++i) {
          const int q = (i & 1) * 64 + lane, r = (j / 3 - 1) * 96 + (idx & 3) * 24 + (q >> 2) % 24;
          const unsigned o = (unsigned)(r * 9216 + ft * 288 + ((q & 3) < 3 ? (q & 3) * 128 : 284));
          asm volatile("global_load_dword %0, %1, %2" : "+v"(sink) : "v"(o), "s"(i < 2 ? c.wg : c.wu) : "memory");
        }
      }
      if (j == 16) ldWD();
      SCHED();
    }
    NOPS();
    SCHED();
  }
  if (wave == 7) {
#pragma unroll
    for (int i = 0; i < 5; ++i) {
      const int r = fr & 3;
      float s2 = r == 0 ? acc[i][1][0] : r == 1 ? acc[i][1][1] : r == 2 ? acc[i][1][2] : acc[i][1][3];
      float mean = acc[i][0][0] * (1.0f / 576.0f);
      float var = fmaxf(s2 * (1.0f / 576.0f) - mean * mean, 0.0f);
      const float den = rb(__builtin_amdgcn_sqrtf(rb(rb(var) + 1e-5f)));
      if (g4 == (fr >> 2)) {
        *(LAS f32x2*)(lds + P1_STAT + (i * 16 + fr) * 8) = (f32x2){rb(mean), __builtin_amdgcn_rcpf(den)};
        if (i) *(LAS float*)(lds + L_MEAN + ((i - 1) * 16 + fr) * 4) = mean;
      }
    }
  }
  int qo[2][2] = {{0, 0}, {0, 0}};
  if (wave < 7) {
#pragma unroll
    for (int T = 0; T < 2; ++T)
#pragma unroll
      for (int hh = 0; hh < 2; ++hh) {
        const int cc = (wave * 2 + T) * 16 + g4 * 4 + hh * 2;
        qo[T][hh] = P1_QOFF + fr * P1_QROW + (cc < 216 ? (cc / 18) * 48 + (cc % 18) * 2 : 11 * 48 + 40);
        PIN(qo[T][hh]);
      }
  }
  WAIT_L0();
  BAR();
  if (wave == 7) { asm volatile("s_setprio 0"); p2_prefetch_w<24>(c, lane, 0, 1); }
  if (wave < 7) {
    f32x2 scv[5]; f32x4 cs[2];
#pragma unroll
    for (int i = 0; i < 5; ++i) scv[i] = *(LAS f32x2*)(lds + P1_STAT + (i * 16 + fr) * 8);
    cs[0] = *(LAS f32x4*)(lds + P1_CS + (wave * 32 + g4 * 4) * 4); cs[1] = *(LAS f32x4*)(lds + P1_CS + (wave * 32 + 16 + g4 * 4) * 4);
    SCHED();
#pragma unroll
    for (int i = 0; i < 5; ++i) {
      const f32x2 sc = scv[i];
#pragma unroll
      for (int T = 0; T < 2; ++T) {
        const f32x4 v = (acc[i][T] + cs[T] * sc[0]) * sc[1];
        *(LAS unsigned*)(lds + qo[T][0] + i * 16 * P1_QROW) = pk2(v[0], v[1]);
        *(LAS unsigned*)(lds + qo[T][1] + i * 16 * P1_QROW) = pk2(v[2], v[3]);
      }
    }
  }
  if (l == 0 && u < 1728) {
    if (wave == 7) WAIT_V(24); else WAIT_V(0);
    ldsp im = lds + P1_OOFF + wave * 3072 + (lane >> 2) * 96 + (lane & 3) * 8;
    const int kb = u / 12, cg = u % 12;
    const unsigned dst = WS_WD + (unsigned)((((cg / 3) * 144 + kb) * 9 + (cg % 3) * 3) * 1024 + lane * 16);
#pragma unroll
    for (int T = 0; T < 3; ++T) stf(rs, dst + T * 1024, cat8(lds_tr(im + T * 32), lds_tr(im + T * 32 + 16 * 96)));
  } else if (wave == 7) WAIT_V(24); else WAIT_V(0);
  asm volatile("" :: "v"(sink));
  if (wave < 7) p2_prefetch_w<4>(c, lane, 24 + wave, 7);
  WAIT_L0();
  BAR();
  {
    const int r = (wave >> 2) * 32 + (lane >> 1), h = wave & 3, hf = tid & 1;
    ldsp rb0 = lds + P1_QOFF + (2 + r) * P1_QROW + h * 48;
    ldsp kb = rb0 + 192 + (hf ? 0 : 8 * P1_QROW);
    ldsp vb = rb0 + 384 + hf * 16;
    u32x4 qa = *(LAS u32x4*)(rb0 + 14 * P1_QROW), qb4 = *(LAS u32x4*)(rb0 + 14 * P1_QROW + 16);
    unsigned qc = *(LAS unsigned*)(rb0 + 14 * P1_QROW + 32);
    u32x4 ka[4], kb4[4]; unsigned kc[4];
#pragma unroll
    for (int w = 0; w < 4; ++w) {
      ka[w] = *(LAS u32x4*)(kb + (6 - 2 * w) * P1_QROW); kb4[w] = *(LAS u32x4*)(kb + (6 - 2 * w) * P1_QROW + 16);
      kc[w] = *(LAS unsigned*)(kb + (6 - 2 * w) * P1_QROW + 32);
    }
    u32x4 va[8]; unsigned vc[8];
#pragma unroll
    for (int w = 0; w < 8; ++w) { va[w] = *(LAS u32x4*)(vb + (14 - 2 * w) * P1_QROW); vc[w] = *(LAS unsigned*)(vb + (14 - 2 * w) * P1_QROW + 16); }
    SCHED();
    float lg[4] = {0.f, 0.f, 0.f, 0.f};
#pragma unroll
    for (int j = 0; j < 4; ++j)
#pragma unroll
      for (int w = 0; w < 4; ++w) { const unsigned qq = qa[j], kk = ka[w][j]; lg[w] = dot2(qq, kk, lg[w]); }
#pragma unroll
    for (int j = 0; j < 4; ++j)
#pragma unroll
      for (int w = 0; w < 4; ++w) { const unsigned qq = qb4[j], kk = kb4[w][j]; lg[w] = dot2(qq, kk, lg[w]); }
#pragma unroll
    for (int w = 0; w < 4; ++w) {
      float d = dot2(qc, kc[w], lg[w]);
      d = rb(rb(d) * 0.23570226039551584f);
      lg[w] = (m0 + r - (hf * 8 + 2 * w) >= 0) ? d : -1e9f;
    }
    float mx = fmaxf(fmaxf(lg[0], lg[1]), fmaxf(lg[2], lg[3]));
    mx = fmaxf(mx, swap1(mx));
    float sum = 0.f;
#pragma unroll
    for (int w = 0; w < 4; ++w) { lg[w] = __expf(lg[w] - mx); sum += lg[w]; }
    sum += swap1(sum);
    const float rs_ = __builtin_amdgcn_rcpf(sum);
    float pw[8];
#pragma unroll
    for (int w = 0; w < 4; ++w) {
      const float mine = rb(lg[w] * rs_), other = swap1(mine);
      pw[w] = hf ? other : mine; pw[4 + w] = hf ? mine : other;
    }
    f32x2 ov[5];
#pragma unroll
    for (int d = 0; d < 5; ++d) ov[d] = (f32x2){0.f, 0.f};
#pragma unroll
    for (int w = 0; w < 8; ++w) {
      const unsigned ph = __float_as_uint(pw[w]), pl = ph >> 16;
#pragma unroll
      for (int j = 0; j < 5; ++j) {
        const unsigned vv = j < 4 ? va[w][j & 3] : vc[w];
        ov[j][0] = dot2(vv, pl, ov[j][0]); ov[j][1] = dot2(vv, ph, ov[j][1]);
      }
    }
    ldsp ob = lds + P1_OOFF + r * 144 + h * 36 + hf * 16;
#pragma unroll
    for (int j = 0; j < 4; ++j) *(LAS unsigned*)(ob + j * 4) = pk2(ov[j][0], ov[j][1]);
    if (hf) *(LAS unsigned*)(ob + 16) = pk2(ov[4][0], ov[4][1]);
    p2_prefetch_w<4>(c, lane, 52 + wave, 8);
  }
  unsigned oa = WS_O + l * XB + frag_off<18>(m0 + lane, hg * 72 + wave * 8), ob2 = WS_O + l * XB + frag_off<18>(m0 + lane, hg * 72 + 64);
  int la = P1_OOFF + lane * 144 + wave * 16;
  PIN(oa); PIN(ob2); PIN(la);
  WAIT_L0();
  BAR();
  {
    const u32x4 v = *(LAS u32x4*)(lds + la);
    if (wave == 7) { const u32x4 v2 = *(LAS u32x4*)(lds + la + 16); st16c(rs, ob2, v2); }
    st16c(rs, oa, v);
  }
  publish<R1>(c.ws, l * C_L + C_S1 + mt * R1, hg);
  if (l == 0 && wave == 1 && c.bid < 216) flag<8>(c.ws, lane, C_WD + (c.bid / 54) * 8, c.bid % 54);
}

__device__ __forceinline__ void phase2(const Ctx& c, rsrc_t rs, int l) {
  int tid = threadIdx.x; asm volatile("" : "+v"(tid));
  const int lane = tid & 63, wave = c.wave;
  ldsp lds = c.lds;
  const int xcd = c.bid & 7, idx = c.bid >> 3;
  const int mt = (xcd >> 1) * 8 + (idx & 7), nt = (xcd & 1) * 4 + (idx >> 3);
  const int m0 = mt * 64, n0 = nt * 72;
  const int fr = lane & 15, g4 = lane >> 4, wm = wave & 3, kh = wave >> 2;
  u32x2 res[5];
  const int row = m0 + wm * 16 + fr;
#pragma unroll
  for (int T = 0; T < 5; ++T) {
    const int cc = (T < 4 || g4 < 2) ? (n0 + T * 16 + g4 * 4) : n0;
    res[T] = ld8c(rs, (l ? WS_X2 : WS_X0) + frag_off<18>(row, cc));
  }
#define P2_PRE 3
  const int t4 = fr < 8 ? ((lane & 48) + fr) * 16 + 8192 : 9216 + (fr & 1) * 64 + g4 * 16;
  auto rdW = [&](int s, int T) -> bf16x8 {
    const int kb = kh * 9 + s;
    return T < 4 ? lds_b128(lds + (kb >> 1) * WO_ST + ((kb & 1) * 4 + T) * 1024 + lane * 16)
                 : lds_b128(lds + (kb >> 1) * WO_ST + t4 + (kb & 1) * 128);
  };
  bf16x8 wf[7][5];
#pragma unroll
  for (int s = 0; s < P2_PRE; ++s)
#pragma unroll
    for (int T = 0; T < 5; ++T) wf[s][T] = rdW(s, T);
  SCHED();
  if (wave == 7) poll(c.ws, l * C_L + C_S1 + mt * R1 + nt % R1, lane & 7);
  BAR();
  bf16x8 xf[9];
#pragma unroll
  for (int s = 0; s < 9; ++s) xf[s] = __builtin_bit_cast(bf16x8, ld16c(rs, WS_O + l * XB + (unsigned)(mt * 73728 + (wm * 18 + kh * 9 + s) * 1024 + lane * 16)));
  SCHED();
#pragma unroll
  for (int s = P2_PRE; s < 7; ++s)
#pragma unroll
    for (int T = 0; T < 5; ++T) wf[s][T] = rdW(s, T);
  SCHED();
  f32x4 acc[5];
#pragma unroll
  for (int T = 0; T < 5; ++T) acc[T] = (f32x4){0, 0, 0, 0};
#pragma unroll
  for (int s = 0; s < 9; ++s) {
#pragma unroll
    for (int T = 0; T < 5; ++T) acc[T] = MFMA(wf[s % 7][T], xf[s], acc[T]);
    if (s < 2) {
#pragma unroll
      for (int T = 0; T < 5; ++T) wf[s][T] = rdW(s + 7, T);
    }
    SCHED();
  }
#pragma unroll
  for (int T = 0; T < 5; ++T) *(LAS f32x4*)(lds + P2_SCR + (wave * 5 + T) * 1024 + lane * 16) = acc[T];
  unsigned xa[3], sta = WS_STAT + l * 131072 + (row * 8 + nt) * 8;
  const unsigned xo = (kh ? WS_X1 : WS_XC) + l * XB;
#pragma unroll
  for (int tp = 0; tp < 2; ++tp) xa[tp] = xo + frag_off<18>(row, n0 + tp * 32 + ((g4 & 1) ? 16 + (g4 - 1) * 4 : g4 * 4));
  xa[2] = xo + frag_off<18>(row, n0 + 64 + (g4 & 1) * 4);
  int sa = P2_SCR + (wave ^ 4) * 5120 + lane * 16;
  float xm = *(LAS float*)(lds + L_MEAN + (wm * 16 + fr) * 4);
  PIN(xa[0]); PIN(xa[1]); PIN(xa[2]); PIN(sa); PIN(sta); PIN(xm);
  const unsigned fo = flag_off(tid, l * C_L + C_S2 + (mt >> 2) * R2, (mt & 3) * 8 + nt);
  WAIT_L0();
  BAR();
  {
    f32x4 o[5];
#pragma unroll
    for (int T = 0; T < 5; ++T) o[T] = *(LAS f32x4*)(lds + sa + T * 1024);
    SCHED();
    f32x2 u[5][2];
    unsigned pk[5][2];
    float om = 0.f;
#pragma unroll
    for (int T = 0; T < 5; ++T) {
      const f32x4 a = acc[T] + o[T];
      if (T == 4) om = a[0] + a[1];
      pk[T][0] = pk2(lo16(res[T][0]) + rb(a[0]), hi16(res[T][0]) + rb(a[1]));
      pk[T][1] = pk2(lo16(res[T][1]) + rb(a[2]), hi16(res[T][1]) + rb(a[3]));
      u[T][0] = (f32x2){lo16(pk[T][0]), hi16(pk[T][0])}; u[T][1] = (f32x2){lo16(pk[T][1]), hi16(pk[T][1])};
    }
    if (kh == 0) {
      const float mb = rb(xm + __uint_as_float(__builtin_amdgcn_permlane32_swap(__float_as_uint(om), __float_as_uint(om), false, false)[1]));
#pragma unroll
      for (int T = 0; T < 5; ++T)
#pragma unroll
        for (int h = 0; h < 2; ++h) { const f32x2 d = u[T][h] - mb; pk[T][h] = pk2(d[0], d[1]); }
    }
#pragma unroll
    for (int tp = 0; tp < 2; ++tp) {
      auto r0 = __builtin_amdgcn_permlane16_swap(pk[2 * tp][0], pk[2 * tp + 1][0], false, false);
      auto r1 = __builtin_amdgcn_permlane16_swap(pk[2 * tp][1], pk[2 * tp + 1][1], false, false);
      u32x4 w; w[0] = r0[0]; w[1] = r1[0]; w[2] = r0[1]; w[3] = r1[1];
      st16c(rs, xa[tp], w);
    }
    if (g4 < 2) { u32x2 w; w[0] = pk[4][0]; w[1] = pk[4][1]; st8c(rs, xa[2], w); }
    if (kh) {
      const float t4m = g4 < 2 ? 1.0f : 0.0f;
      f32x2 s2 = {0.f, 0.f};
#pragma unroll
      for (int T = 0; T < 5; ++T)
#pragma unroll
        for (int h = 0; h < 2; ++h) {
          if (T == 4) u[T][h] = g4 < 2 ? u[T][h] : (f32x2){0.f, 0.f};
          s2 += u[T][h];
        }
      const float mean = sum_g4(s2[0] + s2[1]) * (1.0f / 72.0f);
      f32x2 q2 = {0.f, 0.f};
#pragma unroll
      for (int T = 0; T < 5; ++T)
#pragma unroll
        for (int h = 0; h < 2; ++h) {
          const f32x2 d = u[T][h] - (T == 4 ? mean * t4m : mean);
          q2 += d * d;
        }
      const float q = sum_g4(q2[0] + q2[1]);
      if (g4 == 0) { u32x2 w; w[0] = __float_as_uint(mean); w[1] = __float_as_uint(q); st8c(rs, sta, w); }
    }
    WAIT_V(0);
  }
  BAR();
  flag_at<R2>(c.ws, tid, fo);
}

__device__ __forceinline__ void phase3(const Ctx& c, rsrc_t rs, int l) {
  int tid = threadIdx.x; asm volatile("" : "+v"(tid));
  const int lane = tid & 63, wave = c.wave;
  ldsp lds = c.lds;
  const int xcd = c.bid & 7, idx = c.bid >> 3;
  const int mp = (xcd & 1) * 4 + (idx & 3), ft = (xcd >> 1) * 8 + (idx >> 2);
  const int m0 = mp * 256;
  const int grp = wave >> 2, wm = wave & 3;
  unsigned wo0 = gu_off(wave, lane, ft), wo8 = gu_off(8, lane, ft);
  PIN(wo0); PIN(wo8);
  const char* xb = (const char*)c.ws + WS_XC + l * XB + (size_t)(mp * 16 + wave * 2) * 18432;
  const unsigned l16 = lane * 16;
  auto piece = [&](int jj, int sl, int i) {
    const unsigned wd_ = c.lbase + P3_WOFF + sl * P3_WS;
    if (i < 2) glds16c(xb + i * 18432 + jj * 1024, l16, c.lbase + sl * P3_XS + (wave * 2 + i) * 1024);
    else if (i == 2) glds16((const char*)c.wg + (size_t)jj * 32 * 9216, wo0, wd_ + wave * 1024);
    else if (i == 3) glds16((const char*)c.wu + (size_t)jj * 32 * 9216, wo0, wd_ + P3_UOFF + wave * 1024);
    else if (wave < 2) glds16((const char*)(wave ? c.wu : c.wg) + (size_t)jj * 32 * 9216, wo8, wd_ + (wave ? P3_UOFF : 0) + 8192);
  };
#pragma unroll
  for (int p = 0; p < PW0; ++p) { piece(p, p, 2); piece(p, p, 3); piece(p, p, 4); }
  f32x4 acc[4][9];
#pragma unroll
  for (int i = 0; i < 4; ++i)
#pragma unroll
    for (int T = 0; T < 9; ++T) { acc[i][T] = (f32x4){0, 0, 0, 0}; PIN(acc[i][T]); }
  unsigned so = WS_STAT + l * 131072 + (m0 + (tid & 255)) * 64;
  PIN(so);
  if (wave == 7) poll(c.ws, l * C_L + C_S2 + mp * R2 + ft % R2, lane & 31);
  if (wave == 6 && l == 0) poll(c.ws, C_WD + (xcd >> 1) * 8 + (idx & 7), lane < 54 ? lane : 0);
  BAR();
  piece(0, 0, 0); piece(0, 0, 1);
  f32x4 a = acc[0][0], b = a, cq = a, d = a;
  if (grp == 0)
    asm volatile("buffer_load_dwordx4 %0, %4, %5, 0 offen sc1\n\tbuffer_load_dwordx4 %1, %4, %5, 0 offen offset:16 sc1\n\t"
                 "buffer_load_dwordx4 %2, %4, %5, 0 offen offset:32 sc1\n\tbuffer_load_dwordx4 %3, %4, %5, 0 offen offset:48 sc1"
                 : "+v"(a), "+v"(b), "+v"(cq), "+v"(d) : "v"(so), "s"(rs) : "memory");
#pragma unroll
  for (int p = 1; p < 3; ++p)
#pragma unroll
    for (int i = 0; i < 5; ++i) if (i < 2 || p >= PW0) piece(p, p, i);
  if (PW0 == 1) WAIT_V(8); else WAIT_V(6);
  PIN(a); PIN(b); PIN(cq); PIN(d);
  if (grp == 0) {
    const float mean = ((a[0] + a[2]) + (b[0] + b[2]) + (cq[0] + cq[2]) + (d[0] + d[2])) * 0.125f;
    float m2 = (a[1] + a[3]) + (b[1] + b[3]) + (cq[1] + cq[3]) + (d[1] + d[3]);
    float e0 = a[0] - mean, e1 = a[2] - mean, e2 = b[0] - mean, e3 = b[2] - mean, e4 = cq[0] - mean, e5 = cq[2] - mean, e6 = d[0] - mean, e7 = d[2] - mean;
    m2 += 72.0f * (((e0 * e0 + e1 * e1) + (e2 * e2 + e3 * e3)) + ((e4 * e4 + e5 * e5) + (e6 * e6 + e7 * e7)));
    const float vb = rb(m2 * (1.0f / 576.0f));
    const float den = rb(__builtin_amdgcn_sqrtf(rb(vb + 1e-5f)));
    *(LAS f32x4*)(lds + P3_STAT + tid * 16) = (f32x4){rb(mean), -1.4426950408889634f * __builtin_amdgcn_rcpf(den), den * den, 0.f};
  } else if (tid < 260) *(LAS unsigned*)(lds + P3_CNT + (tid - 256) * 4) = 0u;
  WAIT_L0();
  BAR();
  const int abase = wm * 4096 + lane * 16;
  const int g4_ = lane >> 4, q4 = (lane >> 2) & 3, p4 = lane & 3;
  const int wrow = ((g4_ >> 1) * 16 + (g4_ & 1) * 4 + q4) * 288;
  int wbase[2], mbase[2];
#pragma unroll
  for (int h = 0; h < 2; ++h) {
    wbase[h] = P3_WOFF + h * 2 * P3_WS + wrow + p4 * 8 + (grp ? 160 : 0);
    mbase[h] = P3_WOFF + h * 2 * P3_WS + wrow + (p4 & 1) * 8 + (p4 >> 1) * P3_UOFF + (grp ? 144 : 128);
    PIN(wbase[h]); PIN(mbase[h]);
  }
  auto rdW = [&](int sl, int T) -> bf16x8 {
    const int o = (sl & 1) * P3_WS + (T < 4 ? wbase[sl >> 1] + T * 32 : T < 8 ? wbase[sl >> 1] + P3_UOFF + (T - 4) * 32 : mbase[sl >> 1]);
    return cat8(lds_tr(lds + o), lds_tr(lds + o + 8 * 288));
  };
  auto rdX = [&](int sl, int i) -> bf16x8 { return lds_b128(lds + sl * P3_XS + abase + i * 1024); };
  bf16x8 xf[2][4], wf[4];
#pragma unroll
  for (int i = 0; i < 4; ++i) xf[0][i] = rdX(0, i);
#pragma unroll
  for (int T = 0; T < 3; ++T) wf[T] = rdW(0, T);
#pragma unroll
  for (int i = 0; i < 4; ++i)
#pragma unroll
    for (int T = 0; T < 9; ++T) PIN(acc[i][T]);
  NOPS();
  SCHED();
  auto phase = [&](auto JM, auto LAST, int j) {
    constexpr int jm = decltype(JM)::value;
    constexpr bool last = decltype(LAST)::value;
    constexpr int cur = jm & 1, nx = cur ^ 1, sn = (jm + 1) & 3;
#pragma unroll
    for (int T = 0; T < 9; ++T) {
      if (T + 3 < 9) wf[(9 * jm + T + 3) & 3] = rdW(jm, T + 3);
      else if (!last) wf[(9 * jm + T + 3) & 3] = rdW(sn, T + 3 - 9);
      if (!last && T >= 4 && T < 8) xf[nx][T - 4] = rdX(sn, T - 4);
      if (T >= 4 && j < 15) piece(j + 3, (jm + 3) & 3, T - 4);
#pragma unroll
      for (int i = 0; i < 4; ++i) MFMA_ACC(acc[i][T], wf[(9 * jm + T) & 3], xf[cur][i]);
      if (!last && T == 3) {
        if (j < 16) WAIT_V(4); else WAIT_V(0);
        BAR();
      }
      SCHED();
    }
  };
  typedef std::integral_constant<bool, false> F_t;
#pragma unroll 1
  for (int j = 0; j < 16; j += 4) {
    phase(std::integral_constant<int, 0>{}, F_t{}, j);
    phase(std::integral_constant<int, 1>{}, F_t{}, j + 1);
    phase(std::integral_constant<int, 2>{}, F_t{}, j + 2);
    phase(std::integral_constant<int, 3>{}, F_t{}, j + 3);
  }
  phase(std::integral_constant<int, 0>{}, F_t{}, 16);
  phase(std::integral_constant<int, 1>{}, std::integral_constant<bool, true>{}, 17);
  NOPS();
  SCHED();
  {
  int tz = threadIdx.x; asm volatile("" : "+v"(tz));
  const int fr = tz & 15, g4 = (tz >> 4) & 3;
  f32x4 scv[4];
#pragma unroll
  for (int i = 0; i < 4; ++i) scv[i] = *(LAS f32x4*)(lds + P3_STAT + (wm * 64 + i * 16 + fr) * 16);
  SCHED();
  BAR();
  if (grp) asm volatile("s_setprio 3"); else p4_prefetch_w(c, tz & 63);
  const int fl = l * C_L + (mp * 4 + (ft >> 3)) * R3;
  const unsigned ho = WS_H + l * HB;
  unsigned md[4];
  const int pA = (ft >> 3) * 1152 + (grp ? (ft & 7) * 32 : 576 + (ft & 7) * 64), pB = pA + (grp ? 256 : 32);
  const int pM = (ft >> 3) * 1152 + (grp ? 512 : 1088) + (ft & 7) * 8;
  const int c8 = (g4 & 1) * 16 + (g4 >> 1) * 8;
#pragma unroll
  for (int tp = 0; tp < 2; ++tp) {
#pragma unroll
    for (int i = 0; i < 4; ++i) {
      const int r = wm * 64 + i * 16 + fr;
      const f32x4 sc = scv[i];
      unsigned pk[2][2];
#pragma unroll
      for (int t = 0; t < 2; ++t) {
        const int T = tp * 2 + t;
        const f32x4 tg = acc[i][T];
        const f32x4 tu = acc[i][4 + T];
        const f32x4 ea = tg * sc[1];
        f32x4 e; e[0] = __builtin_amdgcn_exp2f(ea[0]); e[1] = __builtin_amdgcn_exp2f(ea[1]); e[2] = __builtin_amdgcn_exp2f(ea[2]); e[3] = __builtin_amdgcn_exp2f(ea[3]);
        const f32x4 dn = e * sc[2] + sc[2];
        f32x4 rc; rc[0] = __builtin_amdgcn_rcpf(dn[0]); rc[1] = __builtin_amdgcn_rcpf(dn[1]); rc[2] = __builtin_amdgcn_rcpf(dn[2]); rc[3] = __builtin_amdgcn_rcpf(dn[3]);
        const f32x4 hv = (tg * tu) * rc;
        pk[t][0] = pk2(hv[0], hv[1]); pk[t][1] = pk2(hv[2], hv[3]);
      }
      auto r0 = __builtin_amdgcn_permlane16_swap(pk[0][0], pk[1][0], false, false);
      auto r1 = __builtin_amdgcn_permlane16_swap(pk[0][1], pk[1][1], false, false);
      u32x4 w; w[0] = r0[0]; w[1] = r1[0]; w[2] = r0[1]; w[3] = r1[1];
      st16c(rs, ho + frag_off<144>(m0 + r, (tp ? pB : pA) + c8), w);
    }
    SCHED();
  }
  if (grp) {
    WAIT_V(4);
    if (last_wave(lds + P3_CNT, tz & 63)) flag<R3>(c.ws, tz & 63, fl + C_S3, ft & 7);
  }
  SCHED();
#pragma unroll
  for (int i = 0; i < 4; ++i) {
    const f32x4 sc = scv[i];
    const f32x4 tm = acc[i][8];
    auto s0 = __builtin_amdgcn_permlane32_swap(__float_as_uint(tm[0]), __float_as_uint(tm[2]), false, false);
    auto s1 = __builtin_amdgcn_permlane32_swap(__float_as_uint(tm[1]), __float_as_uint(tm[3]), false, false);
    const f32x2 tg = {__uint_as_float(s0[0]), __uint_as_float(s1[0])};
    const f32x2 tu = {__uint_as_float(s0[1]), __uint_as_float(s1[1])};
    const f32x2 ea = tg * sc[1];
    f32x2 e; e[0] = __builtin_amdgcn_exp2f(ea[0]); e[1] = __builtin_amdgcn_exp2f(ea[1]);
    const f32x2 dn = e * sc[2] + sc[2];
    f32x2 rc; rc[0] = __builtin_amdgcn_rcpf(dn[0]); rc[1] = __builtin_amdgcn_rcpf(dn[1]);
    const f32x2 hv = (tg * tu) * rc;
    md[i] = pk2(hv[0], hv[1]);
  }
  {
    auto a0 = __builtin_amdgcn_permlane16_swap(md[0], md[1], false, false);
    auto a1 = __builtin_amdgcn_permlane16_swap(md[2], md[3], false, false);
    auto b0 = __builtin_amdgcn_permlane32_swap(a0[0], a1[0], false, false);
    auto b1 = __builtin_amdgcn_permlane32_swap(a0[1], a1[1], false, false);
    u32x4 w; w[0] = b0[0]; w[1] = b0[1]; w[2] = b1[0]; w[3] = b1[1];
    st16c(rs, ho + frag_off<144>(m0 + wm * 64 + g4 * 16 + fr, pM), w);
  }
  SCHED();
  if (grp) {
    WAIT_V(0);
    if (last_wave(lds + P3_CNT + 4, tz & 63)) flag<R3>(c.ws, tz & 63, fl + C_S3B, ft & 7);
  } else WAIT_V(9);
  }
}

__device__ __forceinline__ void phase4(const Ctx& c, rsrc_t rs, int l, u16* out) {
  int tid = threadIdx.x; asm volatile("" : "+v"(tid));
  const int lane = tid & 63, wave = c.wave;
  ldsp lds = c.lds;
  const int xcd = c.bid & 7, idx = c.bid >> 3;
  const int ks = xcd >> 1, m4 = (xcd & 1) * 8 + (idx & 7), n4 = idx >> 3;
  const int m0 = m4 * 128, n0 = n4 * 144;
  const int tile = m4 * 4 + n4;
  const int fr = lane & 15, g4 = lane >> 4;
  const unsigned so_ = WS_SLAB + l * SLABB;
  const int fl = l * C_L + ((m4 >> 1) * 4 + ks) * R3 + ((m4 & 1) * 4 + n4) % R3;
  const unsigned l16 = lane * 16;
  if (wave >= 4) {
    const int lw = wave - 4;
    const char* hb = (const char*)c.ws + WS_H + l * HB + (size_t)((m4 * 8 + 2 * lw) * 144 + ks * 36) * 1024;
    const char* wb = (const char*)c.ws + WS_WD + (size_t)(n4 * 144 + ks * 36) * 9216 + lw * 1024;
    auto stageX = [&](int st) {
      const unsigned sl = c.lbase + (st & 3) * P4_ST;
#pragma unroll
      for (int i = 0; i < 4; ++i) glds16c(hb + (size_t)st * 2048 + (i >> 1) * (144 * 1024) + (i & 1) * 1024, l16, sl + lw * 4096 + i * 1024);
    };
    auto stageW = [&](int st) {
      const unsigned sl = c.lbase + (st & 3) * P4_ST;
#pragma unroll
      for (int i = 0; i < 4; ++i) glds16c(wb + (size_t)st * 18432 + i * 4096, l16, sl + P4_XS + (lw + 4 * i) * 1024);
      if (lw < 2) glds16c(wb + (size_t)st * 18432 + 16384, l16, sl + P4_XS + (lw + 16) * 1024);
    };
    if (lw == 3) {
      poll(c.ws, fl + C_S3, lane & 7);
      if (lane == 0) *(volatile LAS unsigned*)(lds + P3_CNT + 12) = 1u;
    } else lds_spin(c.lbase + P3_CNT + 12, 1u);
    stageX(0); stageX(1); stageX(2);
    asm volatile("s_setprio 0");
    WAIT_V(0);
    BAR();
#pragma unroll 1
    for (int st = 0; st < 17; ++st) {
      if (st < 16) WAIT_V(8); else WAIT_V(0);
      BAR();
      if (lw == 2 && (st == 0 || st == 5)) poll(c.ws, fl + (st ? C_S3C : C_S3B), lane & 7);
      if (st < 15) { stageX(st + 3); stageW(st + 3); }
    }
    BAR();
  } else {
    const int wm = wave & 1, kg = wave >> 1;
    f32x4 acc[4][9];
    const unsigned s3c = WS_CNT + (unsigned)(l * C_L + C_S3C + (((xcd & 1) * 4 + (idx & 3)) * 4 + (xcd >> 1)) * R3 + (lane & 7)) * 256 + (idx >> 2) * 4;
    const int abase = (wm * 8 + kg) * 1024 + lane * 16;
    const int wbase = P4_XS + kg * 9216 + lane * 16;
    int ab[2][2] = {{abase + kg * 4096, abase + 4096 - kg * 4096}, {abase + kg * 4096 + 2 * P4_ST, abase + 4096 - kg * 4096 + 2 * P4_ST}};
    int wbase2 = wbase + 2 * P4_ST;
    PIN(ab[0][0]); PIN(ab[0][1]); PIN(ab[1][0]); PIN(ab[1][1]); PIN(wbase2);
    auto rdW = [&](int sl, int T) -> bf16x8 { return lds_b128(lds + (sl < 2 ? wbase : wbase2) + (sl & 1) * P4_ST + T * 1024); };
    auto rdX = [&](int sl, int i) -> bf16x8 { return lds_b128(lds + ab[sl >> 1][i >> 1] + (sl & 1) * P4_ST + (i & 1) * 2048); };
    SCHED();
    BAR();
    bf16x8 xf[2][4], wf[6];
#pragma unroll
    for (int i = 0; i < 4; ++i) xf[0][i] = rdX(0, i);
#pragma unroll
    for (int T = 0; T < 5; ++T) wf[T] = rdW(0, T);
    SCHED();
    auto phase = [&](auto JM, auto MODE) {
      constexpr int jm = decltype(JM)::value;
      constexpr bool last = decltype(MODE)::value == 1, first = decltype(MODE)::value == 2;
      constexpr int cur = jm & 1, nx = cur ^ 1, sn = (jm + 1) & 3;
#pragma unroll
      for (int T = 0; T < 9; ++T) {
        if (T + 5 < 9) wf[(9 * jm + T + 5) % 6] = rdW(jm, T + 5);
        else if (!last) wf[(9 * jm + T + 5) % 6] = rdW(sn, T + 5 - 9);
        if (!last && T >= 4 && T < 8) xf[nx][T - 4] = rdX(sn, T - 4);
#pragma unroll
        for (int i = 0; i < 4; ++i) {
          if (first) asm volatile("v_mfma_f32_16x16x32_bf16 %0, %1, %2, 0" : "=&v"(acc[i][T]) : "v"(wf[(9 * jm + T) % 6]), "v"(xf[cur][i]));
          else MFMA_ACC(acc[i][T], wf[(9 * jm + T) % 6], xf[cur][i]);
        }
        if (!last && T == 3) BAR();
        SCHED();
      }
    };
    typedef std::integral_constant<int, 0> I0; typedef std::integral_constant<int, 1> I1; typedef std::integral_constant<int, 2> I2; typedef std::integral_constant<int, 3> I3;
    phase(I0{}, I2{});
#pragma unroll 1
    for (int j = 0; j < 16; j += 4) {
      phase(I1{}, I0{});
      {
        unsigned t_, u_;
        asm volatile("s_cmp_lg_u32 %6, 0\n\ts_cbranch_scc1 1f\n\ts_waitcnt vmcnt(0)\n\ts_mov_b64 exec, 1\n\tds_add_rtn_u32 %0, %2, %4\n\ts_waitcnt lgkmcnt(0)\n\t"
                     "v_readfirstlane_b32 %1, %0\n\ts_mov_b64 exec, 0xff\n\ts_cmp_lg_u32 %1, 3\n\ts_cbranch_scc1 2f\n\tglobal_store_dword %3, %4, %5 sc1\n2:\n\ts_mov_b64 exec, -1\n1:"
                     : "=&v"(t_), "=&s"(u_) : "v"(c.lbase + P3_CNT + 8), "v"(s3c), "v"(1u), "s"(c.ws), "s"(j) : "memory", "scc");
      }
      SCHED();
      phase(I2{}, I0{});
      phase(I3{}, I0{});
      phase(I0{}, I0{});
    }
    phase(I1{}, I1{});
    NOPS();
    SCHED();
#pragma unroll
    for (int i = 0; i < 2; ++i)
#pragma unroll
      for (int T = 0; T < 9; ++T) *(LAS f32x4*)(lds + P4_EX + ((wave * 18 + i * 9 + T) * 1024 + lane * 16)) = acc[2 + i][T];
    const int qd = wm * 2 + kg;
    unsigned so = so_ + (unsigned)(((tile * 4 + qd) * 4 + ks) * 9472 + lane * 16);
    int exo = P4_EX + (wave ^ 2) * 18432 + lane * 16;
    PIN(so); PIN(exo);
    WAIT_L0();
    BAR();
    f32x4 fin[2][9];
#pragma unroll
    for (int i = 0; i < 2; ++i)
#pragma unroll
      for (int T = 0; T < 9; ++T) fin[i][T] = *(LAS f32x4*)(lds + exo + (i * 9 + T) * 1024);
    SCHED();
#pragma unroll
    for (int i = 0; i < 2; ++i)
#pragma unroll
      for (int T = 0; T < 9; ++T) fin[i][T] += acc[i][T];
    if (qd != ks) {
      float mx = 0.f, mx2 = 0.f;
#pragma unroll
      for (int i = 0; i < 2; ++i)
#pragma unroll
        for (int T = 0; T < 9; ++T)
          { mx = amax3(mx, fin[i][T][0], fin[i][T][1]); mx2 = amax3(mx2, fin[i][T][2], fin[i][T][3]); }
      mx = amax3(mx, mx2, mx2);
      const unsigned eb = min(max(__float_as_uint(mx) >> 23, 20u), 254u);
      const float scl = __uint_as_float((268u - eb) << 23);
#pragma unroll
      for (int u = 0; u < 9; ++u) {
        const f32x4 v0 = (u < 8 ? fin[u >> 2][(u & 3) * 2] : fin[0][8]) * scl, v1 = (u < 8 ? fin[u >> 2][(u & 3) * 2 + 1] : fin[1][8]) * scl;
        u32x4 w; w[0] = pkh(v0[0], v0[1]); w[1] = pkh(v0[2], v0[3]); w[2] = pkh(v1[0], v1[1]); w[3] = pkh(v1[2], v1[3]);
        st16c(rs, so + u * 1024, w);
      }
      __builtin_amdgcn_raw_buffer_store_b32((eb - 14u) << 23, rs, so + 9216 - lane * 12, 0, 16);
      const unsigned fo = flag_off(lane, l * C_L + C_S4 + tile * 4 + qd, ks);
      WAIT_V(0);
      flag_at<1>(c.ws, lane, fo);
    } else {
#pragma unroll
      for (int i = 0; i < 2; ++i)
#pragma unroll
        for (int T = 0; T < 9; ++T) *(LAS f32x4*)(lds + P4_OWN + (i * 9 + T) * 1024 + lane * 16) = fin[i][T];
      WAIT_L0();
    }
  }
  u32x2 res[2][2];
#pragma unroll
  for (int uu = 0; uu < 2; ++uu) {
    const int u = uu ? 8 : wave;
    if (uu == 0 || wave == 0) {
#pragma unroll
      for (int t = 0; t < 2; ++t) {
        const int i = u < 8 ? u >> 2 : t, T = u < 8 ? (u & 3) * 2 + t : 8;
        res[uu][t] = ld8c(rs, WS_X1 + l * XB + frag_off<18>(m0 + ks * 32 + i * 16 + fr, n0 + T * 16 + g4 * 4));
      }
    } else { res[uu][0] = (u32x2){0u, 0u}; res[uu][1] = (u32x2){0u, 0u}; }
  }
  unsigned oa[2], sb0 = so_ + (unsigned)((tile * 4 + ks) * 4 * 9472) + lane * 16;
#pragma unroll
  for (int uu = 0; uu < 2; ++uu) {
    const int u = uu ? 8 : wave;
    const int row = m0 + ks * 32 + (u < 8 ? u >> 2 : (g4 & 1)) * 16 + fr;
    const int col = n0 + (u < 8 ? (u & 3) * 32 + (g4 & 1) * 16 : 128) + (g4 >> 1) * 8;
    oa[uu] = l + 1 < NLAYERS ? WS_X2 + frag_off<18>(row, col) : (unsigned)(row * 1152 + col * 2);
    PIN(oa[uu]);
  }
  PIN(sb0);
  if (wave == 4) poll(c.ws, l * C_L + C_S4 + tile * 4 + ks, (lane & 3) == ks ? (ks ^ 1) : (lane & 3));
  BAR();
  float inv[3];
  u32x4 pp[2][3];
#pragma unroll
  for (int v = 0; v < 3; ++v) {
    const int src = v + (v >= ks ? 1 : 0);
    const unsigned sb = sb0 + (unsigned)(src * 9472);
    pp[0][v] = ld16c(rs, sb + wave * 1024);
    inv[v] = __uint_as_float(__builtin_amdgcn_raw_buffer_load_b32(rs, sb + 9216 - lane * 12, 0, 16));
  }
#pragma unroll
  for (int v = 0; v < 3; ++v) pp[1][v] = (u32x4){0u, 0u, 0u, 0u};
  if (wave == 0) {
#pragma unroll
    for (int v = 0; v < 3; ++v) pp[1][v] = ld16c(rs, sb0 + (unsigned)((v + (v >= ks ? 1 : 0)) * 9472) + 8192);
  }
  SCHED();
#pragma unroll
  for (int uu = 0; uu < 2; ++uu) {
    const int u = uu ? 8 : wave;
    if (uu == 0 || wave == 0) {
      const u32x4* p = pp[uu]; f32x4 own[2];
#pragma unroll
      for (int t = 0; t < 2; ++t) {
        const int i = u < 8 ? u >> 2 : t, T = u < 8 ? (u & 3) * 2 + t : 8;
        own[t] = *(LAS f32x4*)(lds + P4_OWN + (i * 9 + T) * 1024 + lane * 16);
      }
      unsigned pk[2][2];
#pragma unroll
      for (int t = 0; t < 2; ++t) {
        f32x4 sm = own[t];
#pragma unroll
        for (int v = 0; v < 3; ++v) {
          const unsigned w0 = p[v][2 * t], w1 = p[v][2 * t + 1];
          sm += (f32x4){h2f(w0 & 0xffffu), h2f(w0 >> 16), h2f(w1 & 0xffffu), h2f(w1 >> 16)} * inv[v];
        }
        pk[t][0] = pk2(lo16(res[uu][t][0]) + rb(sm[0]), hi16(res[uu][t][0]) + rb(sm[1]));
        pk[t][1] = pk2(lo16(res[uu][t][1]) + rb(sm[2]), hi16(res[uu][t][1]) + rb(sm[3]));
      }
      auto r0 = __builtin_amdgcn_permlane16_swap(pk[0][0], pk[1][0], false, false);
      auto r1 = __builtin_amdgcn_permlane16_swap(pk[0][1], pk[1][1], false, false);
      u32x4 w; w[0] = r0[0]; w[1] = r1[0]; w[2] = r0[1]; w[3] = r1[1];
      if (l + 1 < NLAYERS) st16c(rs, oa[uu], w);
      else *(u32x4*)((char*)out + oa[uu]) = w;
    }
  }
}

extern "C" __global__ __launch_bounds__(512, 2) void k_mega(unsigned char* __restrict__ ws, u16* __restrict__ out, const u16* __restrict__ wg, const u16* __restrict__ wu, const u16* __restrict__ wd) {
  extern __shared__ __attribute__((aligned(16))) unsigned char smem_[];
  Ctx c;
  c.ws = ws; c.wg = wg; c.wu = wu; c.wd = wd; c.lds = (ldsp)smem_; c.lbase = (unsigned)(size_t)c.lds;
  c.wave = __builtin_amdgcn_readfirstlane(threadIdx.x >> 6); c.bid = blockIdx.x;
  const rsrc_t rs = __builtin_amdgcn_make_buffer_rsrc(ws, 0, WS_END, 0x00020000);
#define OPAQUE() asm volatile("" : "+s"(c.ws), "+s"(c.wg), "+s"(c.wu), "+s"(c.wd), "+s"(c.wave), "+s"(c.bid), "+s"(c.lbase), "+s"(l))
#pragma unroll 1
  for (int l = 0; l < NLAYERS; ++l) {
    OPAQUE(); phase1(c, rs, l);
    OPAQUE(); phase2(c, rs, l);
    OPAQUE(); phase3(c, rs, l);
    OPAQUE(); phase4(c, rs, l, out);
  }
}

extern "C" __device__ __attribute__((noinline, used)) void mk_end() {}

extern "C" void kernel_launch(void* const* d_in, const int* in_sizes, int n_in, void* d_out, int out_size, void* d_ws, size_t ws_size,
                              hipStream_t stream) {
  (void)hipFuncSetAttribute((const void*)k_prep, hipFuncAttributeMaxDynamicSharedMemorySize, 2 * PREP_WLDS);
  (void)hipFuncSetAttribute((const void*)k_mega, hipFuncAttributeMaxDynamicSharedMemorySize, MK_LDS);
  k_prep<<<256, 64 * PREP_NW, 2 * PREP_WLDS, stream>>>((const u16*)d_in[0], (const u16*)d_in[1], (const u16*)d_in[2], (unsigned char*)d_ws);
  k_mega<<<256, 512, MK_LDS, stream>>>((unsigned char*)d_ws, (u16*)d_out, (const u16*)d_in[4], (const u16*)d_in[5], (const u16*)d_in[6]);
}
```
